```python
import torch

def _softmax(x, axis=-1):
    return torch.softmax(x, dim=axis)
def _gelu(x):
    return torch.nn.functional.gelu(x, approximate="tanh")
def _silu(x):
    return torch.nn.functional.silu(x)
def _sigmoid(x):
    return torch.sigmoid(x)
def _layernorm(x, axis=-1, eps=1e-5):
    m = x.mean(dim=axis, keepdim=True)
    v = x.var(dim=axis, keepdim=True, unbiased=False)
    return (x - m) / torch.sqrt(v + eps)
def _scatter_add(tgt, idx, src, axis=0):
    return tgt.clone().index_add_(axis, idx, src)
def _segment_sum(src, seg, n):
    out = torch.zeros((n,) + src.shape[1:], dtype=src.dtype)
    return out.index_add_(0, seg, src)
def _pad(x, pw):
    flat = tuple(v for pair in reversed(pw) for v in pair)
    return torch.nn.functional.pad(x, flat)

kernel_name = 'standalone__gqa_block_cached__near_ridge'

def setup_inputs():

    _w = (lambda a: (lambda t: t.to(torch.bfloat16) if t.is_floating_point() else t)(torch.as_tensor(a)))
    B, Sq, D = 16, 48, 448
    Hq, Hkv, Sk, Dh = 16, 8, 48, 256
    assert Hq % Hkv == 0, (Hq, Hkv)
    return {
        "x": _w((lambda *s: torch.randn(*s, dtype=torch.bfloat16) * 0.8)(B, Sq, D)),
        "k_cache": _w((lambda *s: torch.randn(*s, dtype=torch.bfloat16) * 0.8)(B, Hkv, Sk, Dh)),
        "v_cache": _w((lambda *s: torch.randn(*s, dtype=torch.bfloat16) * 0.05)(B, Hkv, Sk, Dh)),
        "wq": _w((lambda *s: torch.randn(*s, dtype=torch.bfloat16) * 0.8)(D, Hq * Dh) * (D ** -0.5)),
        "wo": _w((lambda *s: torch.randn(*s, dtype=torch.bfloat16) * 0.05)(Hq * Dh, D) * ((Hq * Dh) ** -0.5)),
    }

def reference(x, k_cache, v_cache, wq, wo):
    B, Sq, D = x.shape
    Hq, Dh = 16, 256
    Hkv = k_cache.shape[1]
    rep = Hq // Hkv
    q = (lambda x, *d: x.permute(*d))((x @ wq).reshape(B, Sq, Hq, Dh), 0, 2, 1, 3)
    k = (lambda x, n, axis: torch.repeat_interleave(x, n, dim=axis))(k_cache, rep, axis=1)
    v = (lambda x, n, axis: torch.repeat_interleave(x, n, dim=axis))(v_cache, rep, axis=1)
    scores = torch.einsum("bhqd,bhkd->bhqk", q, k) * (Dh ** -0.5)
    p = _softmax(scores, axis=-1)
    o = torch.einsum("bhqk,bhkd->bhqd", p, v)
    return (lambda x, *d: x.permute(*d))(o, 0, 2, 1, 3).reshape(B, Sq, Hq * Dh) @ wo
```

```cpp
#include <hip/hip_runtime.h>
#include <hip/hip_bf16.h>
#include <stdint.h>

typedef __attribute__((ext_vector_type(8))) __bf16 bf16x8;
typedef __attribute__((ext_vector_type(2))) __bf16 bf16x2;
typedef __attribute__((ext_vector_type(4))) float f32x4;
typedef __attribute__((ext_vector_type(2))) float f32x2;
typedef __attribute__((ext_vector_type(4))) unsigned u32x4;
typedef __attribute__((ext_vector_type(2))) unsigned u32x2;

#define DI __device__ __forceinline__
#define MFMA(a, b, c) __builtin_amdgcn_mfma_f32_16x16x32_bf16(__builtin_bit_cast(bf16x8, (a)), __builtin_bit_cast(bf16x8, (b)), (c), 0, 0, 0)

constexpr int NB = 16, SQ = 48, DM = 448, HQ = 16, HKV = 8, SK = 48, DH = 256;
constexpr int FRAG = 1536;

DI unsigned pk2(float a, float b) {
  f32x2 v = {a, b};
  bf16x2 r = __builtin_convertvector(v, bf16x2);
  return __builtin_bit_cast(unsigned, r);
}
DI u32x2 pk4(f32x4 v) {
  u32x2 r = {pk2(v[0], v[1]), pk2(v[2], v[3])};
  return r;
}
DI u32x4 cat(u32x2 a, u32x2 b) {
  u32x4 r = {a[0], a[1], b[0], b[1]};
  return r;
}

constexpr int LDS_K = 0;
constexpr int LDS_V = LDS_K + 3 * 8 * 1024;
constexpr int LDS_S = LDS_V + 6 * 8 * 1024;
constexpr int LDS_C = LDS_S + 4 * 9 * 1024;
constexpr int LDS_TOTAL = LDS_C + 16;

__global__ __launch_bounds__(512, 2) void k1_attn(const uint16_t* __restrict__ x, const uint16_t* __restrict__ kc,
                                                  const uint16_t* __restrict__ vc, const uint16_t* __restrict__ wq,
                                                  const uint16_t* __restrict__ wo, char* __restrict__ wsP,
                                                  char* __restrict__ wsVW) {
  __shared__ __attribute__((aligned(16))) char lds[LDS_TOTAL];
  const int bid = blockIdx.x;
  const int g = bid & 7;
  const int idx = bid >> 3;
  const int h = 2 * g + (idx & 1);
  const int b = idx >> 1;
  const int p = b >> 1;
  const int wave = __builtin_amdgcn_readfirstlane(threadIdx.x >> 6);
  const int lane = threadIdx.x & 63;
  const int i = lane & 15, gq = lane >> 4;

  u32x4 st[9];
  {
    const uint16_t* kp = kc + ((size_t)(b * HKV + g) * SK + i) * DH + 32 * wave + 8 * gq;
#pragma unroll
    for (int j = 0; j < 3; ++j) st[j] = *(const u32x4*)(kp + 16 * j * DH);
#pragma unroll
    for (int j = 0; j < 6; ++j) {
      const int bb = j / 3, kt = j % 3;
      const uint16_t* vp = vc + ((size_t)((2 * p + bb) * HKV + g) * SK + 16 * kt + i) * DH + 32 * wave + 8 * gq;
      st[3 + j] = *(const u32x4*)vp;
    }
  }
  if (threadIdx.x == 0) *(volatile unsigned*)(lds + LDS_C) = 0u;

  if (wave < 4) {
    const int w = wave;
    const int t0 = b % 7;
    const uint16_t* wqb[7];
    const uint16_t* xb_[7];
#pragma unroll
    for (int j = 0; j < 7; ++j) {
      int tj = j + t0;
      tj = tj >= 7 ? tj - 7 : tj;
      wqb[j] = wq + (size_t)(112 * w + 16 * tj + i) * (HQ * DH) + h * DH + 8 * gq;
      xb_[j] = x + (size_t)(b * SQ + i) * DM + 112 * w + 16 * tj + 4 * gq;
    }
    u32x4 W[2][2][8];
    u32x2 X[2][2][3];
#pragma unroll
    for (int pr = 0; pr < 2; ++pr)
#pragma unroll
      for (int ks = 0; ks < 8; ++ks)
#pragma unroll
        for (int s = 0; s < 2; ++s) W[pr][s][ks] = *(const u32x4*)(wqb[2 * pr + s] + 32 * ks);
#pragma unroll
    for (int pr = 0; pr < 2; ++pr)
#pragma unroll
      for (int s = 0; s < 2; ++s)
#pragma unroll
        for (int qt = 0; qt < 3; ++qt) X[pr][s][qt] = *(const u32x2*)(xb_[2 * pr + s] + 16 * qt * DM);
    __builtin_amdgcn_sched_barrier(0);
#pragma unroll
    for (int j = 0; j < 3; ++j) *(u32x4*)(lds + LDS_K + (j * 8 + wave) * 1024 + lane * 16) = st[j];
#pragma unroll
    for (int j = 0; j < 6; ++j) *(u32x4*)(lds + LDS_V + (j * 8 + wave) * 1024 + lane * 16) = st[3 + j];
    __syncthreads();

    f32x4 sacc[3][3];
#pragma unroll
    for (int kt = 0; kt < 3; ++kt)
#pragma unroll
      for (int qt = 0; qt < 3; ++qt) sacc[kt][qt] = f32x4{0.f, 0.f, 0.f, 0.f};

#pragma unroll
    for (int pr = 0; pr < 4; ++pr) {
      const int par = pr & 1;
      const int ns = (pr == 3) ? 1 : 2;
      f32x4 acc[2][3];
#pragma unroll
      for (int s = 0; s < 2; ++s)
#pragma unroll
        for (int kt = 0; kt < 3; ++kt) acc[s][kt] = f32x4{0.f, 0.f, 0.f, 0.f};
#pragma unroll
      for (int ks = 0; ks < 8; ++ks) {
        u32x4 kf[3];
#pragma unroll
        for (int kt = 0; kt < 3; ++kt) kf[kt] = *(const u32x4*)(lds + LDS_K + (kt * 8 + ks) * 1024 + lane * 16);
#pragma unroll
        for (int s = 0; s < 2; ++s) {
          if (s < ns) {
#pragma unroll
            for (int kt = 0; kt < 3; ++kt) acc[s][kt] = MFMA(W[par][s][ks], kf[kt], acc[s][kt]);
          }
        }
        if (pr + 2 < 4) {
#pragma unroll
          for (int s = 0; s < 2; ++s) {
            if (2 * (pr + 2) + s < 7) W[par][s][ks] = *(const u32x4*)(wqb[2 * (pr + 2) + s] + 32 * ks);
          }
        }
      }
      u32x4 xf[3];
#pragma unroll
      for (int qt = 0; qt < 3; ++qt) {
        if (ns == 2) xf[qt] = cat(X[par][0][qt], X[par][1][qt]);
        else xf[qt] = cat(X[par][0][qt], u32x2{0u, 0u});
      }
      if (pr + 2 < 4) {
#pragma unroll
        for (int s = 0; s < 2; ++s)
#pragma unroll
          for (int qt = 0; qt < 3; ++qt)
            if (2 * (pr + 2) + s < 7) X[par][s][qt] = *(const u32x2*)(xb_[2 * (pr + 2) + s] + 16 * qt * DM);
      }
#pragma unroll
      for (int kt = 0; kt < 3; ++kt) {
        u32x4 af;
        if (ns == 2) af = cat(pk4(acc[0][kt]), pk4(acc[1][kt]));
        else af = cat(pk4(acc[0][kt]), u32x2{0u, 0u});
#pragma unroll
        for (int qt = 0; qt < 3; ++qt) sacc[kt][qt] = MFMA(af, xf[qt], sacc[kt][qt]);
      }
    }
#pragma unroll
    for (int kt = 0; kt < 3; ++kt)
#pragma unroll
      for (int qt = 0; qt < 3; ++qt) *(f32x4*)(lds + LDS_S + ((w * 3 + qt) * 3 + kt) * 1024 + lane * 16) = sacc[kt][qt];
    __builtin_amdgcn_fence(__ATOMIC_RELEASE, "workgroup");
    unsigned* cnt = (unsigned*)(lds + LDS_C);
    if (lane == 0) __hip_atomic_fetch_add(cnt, 1u, __ATOMIC_RELAXED, __HIP_MEMORY_SCOPE_WORKGROUP);
    if (w < 3) {
      const int qt = w;
      while (__hip_atomic_load(cnt, __ATOMIC_RELAXED, __HIP_MEMORY_SCOPE_WORKGROUP) < 4u) __builtin_amdgcn_s_sleep(1);
      __builtin_amdgcn_fence(__ATOMIC_ACQUIRE, "workgroup");
      f32x4 s[3];
#pragma unroll
      for (int kt = 0; kt < 3; ++kt) {
        s[kt] = *(const f32x4*)(lds + LDS_S + ((0 * 3 + qt) * 3 + kt) * 1024 + lane * 16);
#pragma unroll
        for (int ww = 1; ww < 4; ++ww) s[kt] += *(const f32x4*)(lds + LDS_S + ((ww * 3 + qt) * 3 + kt) * 1024 + lane * 16);
      }
      const float sc = 0.0625f * 1.44269504088896340736f;
      float m = -INFINITY;
#pragma unroll
      for (int kt = 0; kt < 3; ++kt)
#pragma unroll
        for (int r = 0; r < 4; ++r) {
          s[kt][r] *= sc;
          m = fmaxf(m, s[kt][r]);
        }
      m = fmaxf(m, __shfl_xor(m, 16));
      m = fmaxf(m, __shfl_xor(m, 32));
      float sum = 0.f;
#pragma unroll
      for (int kt = 0; kt < 3; ++kt)
#pragma unroll
        for (int r = 0; r < 4; ++r) {
          s[kt][r] = __builtin_amdgcn_exp2f(s[kt][r] - m);
          sum += s[kt][r];
        }
      sum += __shfl_xor(sum, 16);
      sum += __shfl_xor(sum, 32);
      const float inv = 1.0f / sum;
#pragma unroll
      for (int kt = 0; kt < 3; ++kt) s[kt] *= inv;
      char* pp = wsP + (size_t)((b * HQ + h) * 3 + qt) * FRAG;
      *(u32x4*)(pp + lane * 16) = cat(pk4(s[0]), pk4(s[1]));
      *(u32x2*)(pp + 1024 + lane * 8) = pk4(s[2]);
    }
  } else {
    const int u = wave - 4;
    const int blk = (b & 1) ? 4 + u : u;
    const bool active = blk < 7;
    const int blkc = active ? blk : 0;
    constexpr int WIN = 6;
    const uint16_t* wob = wo + (size_t)(h * DH + 8 * gq) * DM + 64 * blkc + 4 * i;
    u32x2 R[8][8];
    if (active) {
#pragma unroll
      for (int ks = 0; ks < WIN; ++ks) {
        const int kr = (ks + p) & 7;
#pragma unroll
        for (int j = 0; j < 8; ++j) R[ks][j] = *(const u32x2*)(wob + (size_t)(32 * kr + j) * DM);
      }
    }
    __builtin_amdgcn_sched_barrier(0);
#pragma unroll
    for (int j = 0; j < 3; ++j) *(u32x4*)(lds + LDS_K + (j * 8 + wave) * 1024 + lane * 16) = st[j];
#pragma unroll
    for (int j = 0; j < 6; ++j) *(u32x4*)(lds + LDS_V + (j * 8 + wave) * 1024 + lane * 16) = st[3 + j];
    __syncthreads();
    if (active) {
      f32x4 acc[6][4];
#pragma unroll
      for (int mt = 0; mt < 6; ++mt)
#pragma unroll
        for (int c = 0; c < 4; ++c) acc[mt][c] = f32x4{0.f, 0.f, 0.f, 0.f};
#pragma unroll
      for (int ks = 0; ks < 8; ++ks) {
        const int kr = (ks + p) & 7;
        u32x4 bf[4];
#pragma unroll
        for (int jj = 0; jj < 4; ++jj) {
          bf[0][jj] = __builtin_amdgcn_perm(R[ks][2 * jj + 1][0], R[ks][2 * jj][0], 0x05040100u);
          bf[1][jj] = __builtin_amdgcn_perm(R[ks][2 * jj + 1][0], R[ks][2 * jj][0], 0x07060302u);
          bf[2][jj] = __builtin_amdgcn_perm(R[ks][2 * jj + 1][1], R[ks][2 * jj][1], 0x05040100u);
          bf[3][jj] = __builtin_amdgcn_perm(R[ks][2 * jj + 1][1], R[ks][2 * jj][1], 0x07060302u);
        }
        if (ks + WIN < 8) {
          const int kr2 = (ks + WIN + p) & 7;
#pragma unroll
          for (int j = 0; j < 8; ++j) R[ks + WIN][j] = *(const u32x2*)(wob + (size_t)(32 * kr2 + j) * DM);
        }
#pragma unroll
        for (int mt = 0; mt < 6; ++mt) {
          const u32x4 va = *(const u32x4*)(lds + LDS_V + (mt * 8 + kr) * 1024 + lane * 16);
#pragma unroll
          for (int c = 0; c < 4; ++c) acc[mt][c] = MFMA(va, bf[c], acc[mt][c]);
        }
      }
#pragma unroll
      for (int bb = 0; bb < 2; ++bb)
#pragma unroll
        for (int c = 0; c < 4; ++c) {
          char* vp = wsVW + (size_t)((((2 * p + bb) * HQ + h) * 7 + blk) * 4 + c) * FRAG;
          *(u32x4*)(vp + lane * 16) = cat(pk4(acc[3 * bb + 0][c]), pk4(acc[3 * bb + 1][c]));
          *(u32x2*)(vp + 1024 + lane * 8) = pk4(acc[3 * bb + 2][c]);
        }
    }
  }
}

__global__ __launch_bounds__(512, 2) void k2_out(const char* __restrict__ wsP, const char* __restrict__ wsVW,
                                                 uint16_t* __restrict__ out) {
  __shared__ __attribute__((aligned(16))) char lds[8 * 6 * 1024];
  const int bid = blockIdx.x;
  const int rest = bid >> 3;
  const int b = (bid & 7) + 8 * (rest / 14);
  const int t = rest % 14;
  const int blk = t >> 1, cp = t & 1;
  const int wave = __builtin_amdgcn_readfirstlane(threadIdx.x >> 6);
  const int lane = threadIdx.x & 63;
  const int i = lane & 15, gq = lane >> 4;

  u32x4 p16[2][3], w16[2][2];
  u32x2 p8[2][3], w8[2][2];
#pragma unroll
  for (int hh = 0; hh < 2; ++hh) {
    const int h = 2 * wave + hh;
#pragma unroll
    for (int cc = 0; cc < 2; ++cc) {
      const char* vp = wsVW + (size_t)(((b * HQ + h) * 7 + blk) * 4 + 2 * cp + cc) * FRAG;
      w16[hh][cc] = *(const u32x4*)(vp + lane * 16);
      w8[hh][cc] = *(const u32x2*)(vp + 1024 + lane * 8);
    }
#pragma unroll
    for (int qt = 0; qt < 3; ++qt) {
      const char* pp = wsP + (size_t)((b * HQ + h) * 3 + qt) * FRAG;
      p16[hh][qt] = *(const u32x4*)(pp + lane * 16);
      p8[hh][qt] = *(const u32x2*)(pp + 1024 + lane * 8);
    }
  }
  f32x4 acc[3][2];
#pragma unroll
  for (int qt = 0; qt < 3; ++qt)
#pragma unroll
    for (int cc = 0; cc < 2; ++cc) {
      f32x4 a = f32x4{0.f, 0.f, 0.f, 0.f};
      a = MFMA(p16[0][qt], w16[0][cc], a);
      a = MFMA(p16[1][qt], w16[1][cc], a);
      a = MFMA(cat(p8[0][qt], p8[1][qt]), cat(w8[0][cc], w8[1][cc]), a);
      acc[qt][cc] = a;
    }
#pragma unroll
  for (int qt = 0; qt < 3; ++qt)
#pragma unroll
    for (int cc = 0; cc < 2; ++cc) *(f32x4*)(lds + ((wave * 3 + qt) * 2 + cc) * 1024 + lane * 16) = acc[qt][cc];
  __syncthreads();
  if (wave < 3) {
    const int qt = wave;
    f32x4 s0 = f32x4{0.f, 0.f, 0.f, 0.f}, s1 = s0;
#pragma unroll
    for (int ww = 0; ww < 8; ++ww) {
      s0 += *(const f32x4*)(lds + ((ww * 3 + qt) * 2 + 0) * 1024 + lane * 16);
      s1 += *(const f32x4*)(lds + ((ww * 3 + qt) * 2 + 1) * 1024 + lane * 16);
    }
#pragma unroll
    for (int r = 0; r < 4; ++r) {
      uint16_t* op = out + (size_t)(b * SQ + 16 * qt + 4 * gq + r) * DM + 64 * blk + 4 * i + 2 * cp;
      *(unsigned*)op = pk2(s0[r], s1[r]);
    }
  }
}

extern "C" void kernel_launch(void* const* d_in, const int* in_sizes, int n_in, void* d_out, int out_size, void* d_ws,
                              size_t ws_size, hipStream_t stream) {
  const uint16_t* x = (const uint16_t*)d_in[0];
  const uint16_t* kc = (const uint16_t*)d_in[1];
  const uint16_t* vc = (const uint16_t*)d_in[2];
  const uint16_t* wq = (const uint16_t*)d_in[3];
  const uint16_t* wo = (const uint16_t*)d_in[4];
  char* wsP = (char*)d_ws;
  char* wsVW = wsP + (size_t)NB * HQ * 3 * FRAG;
  k1_attn<<<256, 512, 0, stream>>>(x, kc, vc, wq, wo, wsP, wsVW);
  k2_out<<<224, 512, 0, stream>>>(wsP, wsVW, (uint16_t*)d_out);
}
```

```cpp
#include <hip/hip_runtime.h>
#include <hip/hip_bf16.h>
#include <stdint.h>

typedef __attribute__((ext_vector_type(8))) __bf16 bf16x8;
typedef __attribute__((ext_vector_type(2))) __bf16 bf16x2;
typedef __attribute__((ext_vector_type(4))) float f32x4;
typedef __attribute__((ext_vector_type(2))) float f32x2;
typedef __attribute__((ext_vector_type(4))) unsigned u32x4;
typedef __attribute__((ext_vector_type(2))) unsigned u32x2;

#define DI __device__ __forceinline__
#define MFMA(a, b, c) __builtin_amdgcn_mfma_f32_16x16x32_bf16(__builtin_bit_cast(bf16x8, (a)), __builtin_bit_cast(bf16x8, (b)), (c), 0, 0, 0)
#define SB() __builtin_amdgcn_sched_barrier(0)

constexpr int NB = 16, SQ = 48, DM = 448, HQ = 16, HKV = 8, SK = 48, DH = 256;

constexpr size_t WS_PA = 0;
constexpr size_t WS_PB = WS_PA + (size_t)NB * HQ * 3 * 1024;
constexpr size_t WS_VA = WS_PB + (size_t)NB * 8 * 3 * 1024;
constexpr size_t WS_VB = WS_VA + (size_t)NB * HQ * 28 * 1024;

DI unsigned pk2(float a, float b) {
  f32x2 v = {a, b};
  bf16x2 r = __builtin_convertvector(v, bf16x2);
  return __builtin_bit_cast(unsigned, r);
}
DI u32x2 pk4(f32x4 v) {
  u32x2 r = {pk2(v[0], v[1]), pk2(v[2], v[3])};
  return r;
}
DI u32x4 cat(u32x2 a, u32x2 b) {
  u32x4 r = {a[0], a[1], b[0], b[1]};
  return r;
}
DI int swap02(int i) { return (i & 10) | ((i & 1) << 2) | ((i >> 2) & 1); }
DI void st16_wt(char* p, u32x4 v) { asm volatile("global_store_dwordx4 %0, %1, off sc1\n\ts_nop 1" ::"v"(p), "v"(v) : "memory"); }
DI void st8_wt(char* p, u32x2 v) { asm volatile("global_store_dwordx2 %0, %1, off sc1\n\ts_nop 1" ::"v"(p), "v"(v) : "memory"); }

DI float red_max(float m) {
  unsigned u = __builtin_bit_cast(unsigned, m);
  auto r = __builtin_amdgcn_permlane32_swap(u, u, false, false);
  m = fmaxf(__builtin_bit_cast(float, (unsigned)r[0]), __builtin_bit_cast(float, (unsigned)r[1]));
  u = __builtin_bit_cast(unsigned, m);
  auto r2 = __builtin_amdgcn_permlane16_swap(u, u, false, false);
  return fmaxf(__builtin_bit_cast(float, (unsigned)r2[0]), __builtin_bit_cast(float, (unsigned)r2[1]));
}
DI float red_sum(float m) {
  unsigned u = __builtin_bit_cast(unsigned, m);
  auto r = __builtin_amdgcn_permlane32_swap(u, u, false, false);
  m = __builtin_bit_cast(float, (unsigned)r[0]) + __builtin_bit_cast(float, (unsigned)r[1]);
  u = __builtin_bit_cast(unsigned, m);
  auto r2 = __builtin_amdgcn_permlane16_swap(u, u, false, false);
  return __builtin_bit_cast(float, (unsigned)r2[0]) + __builtin_bit_cast(float, (unsigned)r2[1]);
}

DI void transpose8(const u32x4 (&R)[8], u32x4 (&F)[8]) {
#pragma unroll
  for (int t = 0; t < 4; ++t)
#pragma unroll
    for (int jj = 0; jj < 4; ++jj) {
      F[2 * t][jj] = __builtin_amdgcn_perm(R[2 * jj + 1][t], R[2 * jj][t], 0x05040100u);
      F[2 * t + 1][jj] = __builtin_amdgcn_perm(R[2 * jj + 1][t], R[2 * jj][t], 0x07060302u);
    }
}

constexpr int LDS_X = 0;
constexpr int LDS_K = 96 * 896;
constexpr int LDS_V = 0;
constexpr int LDS_TOTAL = LDS_K + 96 * 512;

DI void gld16(u32x4& dst, const char* sbase, unsigned voff) {
  asm volatile("global_load_dwordx4 %0, %1, %2" : "=v"(dst) : "v"(voff), "s"(sbase) : "memory");
}
DI void dma16(unsigned lds_dst, const char* sbase, unsigned voff) {
  unsigned keep;
  asm volatile("s_mov_b32 %0, m0\n\ts_mov_b32 m0, %3\n\ts_nop 0\n\tglobal_load_lds_dwordx4 %1, %2\n\ts_mov_b32 m0, %0"
               : "=&s"(keep) : "v"(voff), "s"(sbase), "s"(lds_dst) : "memory");
}
template <int N>
DI void wait_vm() { asm volatile("s_waitcnt vmcnt(%0)" ::"i"(N) : "memory"); }
template <int N>
DI void wait_vm8(u32x4 (&R)[8]) {
  asm volatile("s_waitcnt vmcnt(%8)"
               : "+v"(R[0]), "+v"(R[1]), "+v"(R[2]), "+v"(R[3]), "+v"(R[4]), "+v"(R[5]), "+v"(R[6]), "+v"(R[7])
               : "i"(N) : "memory");
}

template <int OFF>
DI void gld16o(u32x4& dst, const char* sbase, unsigned voff) {
  asm volatile("global_load_dwordx4 %0, %1, %2 offset:%3" : "=v"(dst) : "v"(voff), "s"(sbase), "i"(OFF) : "memory");
}
DI void transpose_q(const u32x4 (&R)[8], u32x4 (&F)[8], int t) {
#pragma unroll
  for (int jj = 0; jj < 4; ++jj) {
    F[2 * t][jj] = __builtin_amdgcn_perm(R[2 * jj + 1][t], R[2 * jj][t], 0x05040100u);
    F[2 * t + 1][jj] = __builtin_amdgcn_perm(R[2 * jj + 1][t], R[2 * jj][t], 0x07060302u);
  }
}

__global__ __launch_bounds__(256, 1) void k1_attn(const uint16_t* __restrict__ x, const uint16_t* __restrict__ kc,
                                                  const uint16_t* __restrict__ vc, const uint16_t* __restrict__ wq,
                                                  const uint16_t* __restrict__ wo, char* __restrict__ ws) {
  __shared__ __attribute__((aligned(16))) char lds[LDS_TOTAL];
  const int bid = blockIdx.x;
  const int g = bid & 7;
  const int idx = bid >> 3;
  const int p = (idx & 15) >> 1;
  const int wave = __builtin_amdgcn_readfirstlane(threadIdx.x >> 6);
  const int lane = threadIdx.x & 63;
  const int i = lane & 15, gq = lane >> 4;
  const int l8 = lane >> 3, e8 = lane & 7;
  const unsigned lds0 = (unsigned)(size_t)lds;
  if (idx < 16) {
    const int h = 2 * g + (idx & 1);
    const int half = wave & 1, kh = wave >> 1;
    const int prot = p % 3;
    auto line_of = [&](int t) {
      int r = t + prot;
      r = r >= 3 ? r - 3 : r;
      return 3 * kh + r;
    };
    auto ks_of = [&](int s) { return s < 6 ? 2 * line_of(s >> 1) + (s & 1) : 12 + kh; };
    const char* xs = (const char*)(x + (size_t)(2 * p) * SQ * DM);
    const unsigned xoff0 = (unsigned)(l8 * 896 + ((e8 ^ (l8 >> 1)) * 16));
    const unsigned xoff1 = (unsigned)(l8 * 896 + ((e8 ^ ((l8 >> 1) | 4)) * 16));
    auto xdma = [&](int L, int rg) { dma16(lds0 + LDS_X + L * 12288 + rg * 1024, xs + (size_t)rg * 8 * 896 + L * 128, (rg & 1) ? xoff1 : xoff0); };
    const char* kbase = (const char*)(kc + (size_t)((2 * p + (wave >> 1)) * HKV + g) * SK * DH) + (wave & 1) * 12288;
    auto kdma = [&](int n) {
      const int row = (wave * 12 + n) * 2 + (lane >> 5);
      dma16(lds0 + LDS_K + (wave * 12 + n) * 1024, kbase + n * 1024, (unsigned)((lane >> 5) * 512 + (((lane & 31) ^ swap02(row & 15)) * 16)));
    };
    unsigned woff[8];
#pragma unroll
    for (int j = 0; j < 8; ++j) woff[j] = (unsigned)((8 * gq + j) * (HQ * DH) + 8 * i) * 2u;
    const char* wbase = (const char*)(wq + h * DH + 128 * half);
    u32x4 R[3][8];
    auto wld = [&](int s, int j) { gld16(R[s % 3][j], wbase + (size_t)(32 * ks_of(s)) * (HQ * DH * 2), woff[j]); };
    auto xaddr = [&](int s, int qt) {
      const int L = s < 6 ? line_of(s >> 1) : 6;
      const int eo = s < 6 ? 4 * (s & 1) : 4 * kh;
      const int row = 16 * qt + i;
      return lds + LDS_X + L * 12288 + row * 128 + (((eo + gq) ^ ((row >> 1) & 7)) * 16);
    };
#pragma unroll
    for (int n = 0; n < 6; ++n) xdma(line_of(0), 6 * half + n);
#pragma unroll
    for (int j = 0; j < 8; ++j) wld(0, j);
    wait_vm<8>();
    __syncthreads();
#pragma unroll
    for (int j = 0; j < 8; ++j) wld(1, j);

    f32x4 acc[8][6];
#pragma unroll
    for (int c = 0; c < 8; ++c)
#pragma unroll
      for (int qt = 0; qt < 6; ++qt) acc[c][qt] = f32x4{0.f, 0.f, 0.f, 0.f};
    u32x4 F[2][8];
    u32x4 xb[2];
    xb[0] = *(const u32x4*)xaddr(0, 0);
    wait_vm8<8>(R[0]);
    transpose8(R[0], F[0]);
    SB();
#pragma unroll
    for (int s = 0; s < 7; ++s) {
#pragma unroll
      for (int qt = 0; qt < 6; ++qt) {
        const bool bar_after = (qt == 5) && (s == 1 || s == 3 || s == 5);
        if (qt < 5) xb[(qt + 1) & 1] = *(const u32x4*)xaddr(s, qt + 1);
        else if (s < 6 && !bar_after) xb[0] = *(const u32x4*)xaddr(s + 1, 0);
        if (qt == 2 && s < 6) {
          if (s == 0) wait_vm8<4>(R[1]);
          if (s == 1) wait_vm8<10>(R[2]);
          if (s == 2) wait_vm8<10>(R[0]);
          if (s == 3) wait_vm8<7>(R[1]);
          if (s == 4) wait_vm8<8>(R[2]);
          if (s == 5) wait_vm8<8>(R[0]);
        }
        if (qt < 4 && s < 5) { wld(s + 2, 2 * qt); wld(s + 2, 2 * qt + 1); }
        if (qt >= 4 && s == 0) { for (int n = 0; n < 3; ++n) xdma(line_of(1), 6 * half + 3 * (qt - 4) + n); }
        if (qt >= 4 && s == 1) { for (int n = 0; n < 3; ++n) xdma(line_of(2), 6 * half + 3 * (qt - 4) + n); }
        if (qt == 4 && s == 2) { for (int n = 0; n < 3; ++n) xdma(6, 3 * wave + n); }
        if (qt >= 4 && (s == 3 || s == 4)) { kdma(4 * (s - 3) + 2 * (qt - 4)); kdma(4 * (s - 3) + 2 * (qt - 4) + 1); }
        if (qt < 2 && s == 5) { kdma(8 + 2 * qt); kdma(8 + 2 * qt + 1); }
        if (qt >= 2 && s < 6) transpose_q(R[(s + 1) % 3], F[(s + 1) & 1], qt - 2);
#pragma unroll
        for (int c = 0; c < 8; ++c) acc[c][qt] = MFMA(F[s & 1][c], xb[qt & 1], acc[c][qt]);
        if (!(s == 6)) SB();
        if (bar_after) {
          if (s == 1) wait_vm<14>();
          if (s == 5) wait_vm<0>();
          __syncthreads();
          xb[0] = *(const u32x4*)xaddr(s + 1, 0);
        }
      }
    }
#pragma unroll
    for (int bb = 0; bb < 2; ++bb) {
      u32x4 kf[3][4];
#pragma unroll
      for (int kt = 0; kt < 3; ++kt)
#pragma unroll
        for (int s = 0; s < 4; ++s) {
          const int row = 48 * bb + 16 * kt + i;
          kf[kt][s] = *(const u32x4*)(lds + LDS_K + row * 512 + (((16 * half + 4 * gq + s) ^ swap02(i)) * 16));
        }
#pragma unroll
      for (int qt = 0; qt < 3; ++qt) {
        const int q6 = 3 * bb + qt;
        f32x4 sacc[3];
#pragma unroll
        for (int kt = 0; kt < 3; ++kt) sacc[kt] = f32x4{0.f, 0.f, 0.f, 0.f};
#pragma unroll
        for (int s = 0; s < 4; ++s) {
          u32x4 qf = {pk2(acc[0][q6][s], acc[1][q6][s]), pk2(acc[2][q6][s], acc[3][q6][s]),
                      pk2(acc[4][q6][s], acc[5][q6][s]), pk2(acc[6][q6][s], acc[7][q6][s])};
#pragma unroll
          for (int kt = 0; kt < 3; ++kt) sacc[kt] = MFMA(kf[kt][s], qf, sacc[kt]);
        }
#pragma unroll
        for (int kt = 0; kt < 3; ++kt)
          *(f32x4*)(lds + LDS_X + (wave * 18 + (bb * 3 + qt) * 3 + kt) * 1024 + lane * 16) = sacc[kt];
      }
    }
    __syncthreads();
    {
      f32x4 s[2][3];
      const int u1 = wave < 2 ? wave + 4 : wave;
#pragma unroll
      for (int rep = 0; rep < 2; ++rep) {
        const int u = rep ? u1 : wave;
#pragma unroll
        for (int kt = 0; kt < 3; ++kt) {
          s[rep][kt] = *(const f32x4*)(lds + LDS_X + (0 * 18 + u * 3 + kt) * 1024 + lane * 16);
#pragma unroll
          for (int ww = 1; ww < 4; ++ww) s[rep][kt] += *(const f32x4*)(lds + LDS_X + (ww * 18 + u * 3 + kt) * 1024 + lane * 16);
        }
      }
      const float sc = 0.0625f * 1.44269504088896340736f;
#pragma unroll
      for (int rep = 0; rep < 2; ++rep) {
        float m = -INFINITY;
#pragma unroll
        for (int kt = 0; kt < 3; ++kt)
#pragma unroll
          for (int r = 0; r < 4; ++r) m = fmaxf(m, s[rep][kt][r]);
        m = red_max(m) * sc;
        float sum = 0.f;
#pragma unroll
        for (int kt = 0; kt < 3; ++kt)
#pragma unroll
          for (int r = 0; r < 4; ++r) {
            s[rep][kt][r] = __builtin_amdgcn_exp2f(__builtin_fmaf(s[rep][kt][r], sc, -m));
            sum += s[rep][kt][r];
          }
        sum = red_sum(sum);
        const float inv = 1.0f / sum;
#pragma unroll
        for (int kt = 0; kt < 3; ++kt) s[rep][kt] *= inv;
      }
#pragma unroll
      for (int rep = 0; rep < 2; ++rep) {
        if (rep == 0 || wave < 2) {
          const int u = rep ? u1 : wave;
          const int bb = u >= 3 ? 1 : 0, qt = u - 3 * bb;
          const int b = 2 * p + bb;
          st16_wt(ws + WS_PA + (size_t)((b * HQ + h) * 3 + qt) * 1024 + lane * 16, cat(pk4(s[rep][0]), pk4(s[rep][1])));
          st8_wt(ws + WS_PB + (size_t)((b * 8 + g) * 3 + qt) * 1024 + (((2 * (h & 1) + (gq >> 1)) << 4) | i) * 16 + (gq & 1) * 8, pk4(s[rep][2]));
        }
      }
    }
  } else {
    const int j = 4 * (idx & 1) + wave;
    const bool active = j < 7;
    auto line_of = [&](int t) { return (t + p) & 3; };
    const unsigned voff0 = (unsigned)(l8 * 512 + ((e8 ^ (l8 >> 1)) * 16));
    const unsigned voff1 = (unsigned)(l8 * 512 + ((e8 ^ ((l8 >> 1) | 4)) * 16));
    const char* vs_ = (const char*)(vc + (size_t)((2 * p + (wave >> 1)) * HKV + g) * SK * DH);
    auto vdma = [&](int L, int n) {
      const int rg = 3 * wave + n;
      dma16(lds0 + LDS_V + L * 12288 + rg * 1024, vs_ + (size_t)(3 * (wave & 1) + n) * 8 * 512 + L * 128, (rg & 1) ? voff1 : voff0);
    };
    if (!active) {
#pragma unroll
      for (int t = 0; t < 4; ++t)
#pragma unroll
        for (int n = 0; n < 3; ++n) vdma(line_of(t), n);
      wait_vm<9>();
      __syncthreads();
      wait_vm<6>();
      __syncthreads();
      wait_vm<0>();
      __syncthreads();
    } else {
      const int np = 128 * j + 8 * i;
      const int hl = np >= DM ? 1 : 0;
      const int n0 = np - DM * hl;
      const unsigned woffa = (unsigned)((hl * DH + 8 * gq) * DM + n0) * 2u;
      const unsigned woffb = woffa + 4 * DM * 2;
      const char* wbase = (const char*)(wo + (size_t)(2 * g) * DH * DM);
      u32x4 R[3][8];
      auto wld = [&](int s, int jr) {
        const char* sb = wbase + (size_t)(32 * (2 * line_of(s >> 1) + (s & 1))) * (DM * 2);
        if (jr == 0) gld16o<0>(R[s % 3][0], sb, woffa);
        if (jr == 1) gld16o<896>(R[s % 3][1], sb, woffa);
        if (jr == 2) gld16o<1792>(R[s % 3][2], sb, woffa);
        if (jr == 3) gld16o<2688>(R[s % 3][3], sb, woffa);
        if (jr == 4) gld16o<0>(R[s % 3][4], sb, woffb);
        if (jr == 5) gld16o<896>(R[s % 3][5], sb, woffb);
        if (jr == 6) gld16o<1792>(R[s % 3][6], sb, woffb);
        if (jr == 7) gld16o<2688>(R[s % 3][7], sb, woffb);
      };
      auto vaddr = [&](int s, int mt) {
        const int row = 16 * mt + i;
        return lds + LDS_V + line_of(s >> 1) * 12288 + row * 128 + (((4 * (s & 1) + gq) ^ ((row >> 1) & 7)) * 16);
      };
#pragma unroll
      for (int n = 0; n < 3; ++n) vdma(line_of(0), n);
#pragma unroll
      for (int jr = 0; jr < 8; ++jr) wld(0, jr);
      wait_vm<8>();
      __syncthreads();
#pragma unroll
      for (int jr = 0; jr < 8; ++jr) wld(1, jr);
      f32x4 acc[6][8];
#pragma unroll
      for (int mt = 0; mt < 6; ++mt)
#pragma unroll
        for (int c = 0; c < 8; ++c) acc[mt][c] = f32x4{0.f, 0.f, 0.f, 0.f};
      u32x4 F[2][8];
      u32x4 va[2];
      va[0] = *(const u32x4*)vaddr(0, 0);
      wait_vm8<8>(R[0]);
      transpose8(R[0], F[0]);
      SB();
#pragma unroll
      for (int s = 0; s < 8; ++s) {
#pragma unroll
        for (int mt = 0; mt < 6; ++mt) {
          const bool bar_after = (mt == 5) && (s == 1 || s == 3);
          if (mt < 5) va[(mt + 1) & 1] = *(const u32x4*)vaddr(s, mt + 1);
          else if (s < 7 && !bar_after) va[0] = *(const u32x4*)vaddr(s + 1, 0);
          if (mt == 2 && s < 7) {
            if (s == 0) wait_vm8<4>(R[1]);
            if (s == 1) wait_vm8<7>(R[2]);
            if (s == 2) wait_vm8<7>(R[0]);
            if (s == 3) wait_vm8<7>(R[1]);
            if (s == 4) wait_vm8<4>(R[2]);
            if (s == 5) wait_vm8<4>(R[0]);
            if (s == 6) wait_vm8<0>(R[1]);
          }
          if (mt < 4 && s < 6) { wld(s + 2, 2 * mt); wld(s + 2, 2 * mt + 1); }
          if (mt == 4 && s < 3) { for (int n = 0; n < 3; ++n) vdma(line_of(s + 1), n); }
          if (mt >= 2 && s < 7) transpose_q(R[(s + 1) % 3], F[(s + 1) & 1], mt - 2);
#pragma unroll
          for (int c = 0; c < 8; ++c) acc[mt][c] = MFMA(va[mt & 1], F[s & 1][c], acc[mt][c]);
          if (!(s == 7)) SB();
          if (bar_after) {
            if (s == 1) wait_vm<11>();
            if (s == 3) wait_vm<8>();
            __syncthreads();
            va[0] = *(const u32x4*)vaddr(s + 1, 0);
          }
        }
      }
      const int m = n0 >> 6, ip = (n0 >> 3) & 7;
      const int h = 2 * g + hl;
      char* va_base = ws + WS_VA + (size_t)(((2 * p * HQ + h) * 7 + m) * 4) * 1024 + ((gq << 4) | ip) * 16;
      char* vb_base = ws + WS_VB + (size_t)(((2 * p * 8 + g) * 7 + m) * 4) * 1024 + (((2 * hl + (gq >> 1)) << 4) | ((gq & 1) << 3) | ip) * 16;
#pragma unroll
      for (int bb = 0; bb < 2; ++bb)
#pragma unroll
        for (int c2 = 0; c2 < 4; ++c2) {
#pragma unroll
          for (int e = 0; e < 2; ++e)
            st16_wt(va_base + (size_t)bb * HQ * 28 * 1024 + c2 * 1024 + e * 128,
                    cat(pk4(acc[3 * bb + 0][2 * c2 + e]), pk4(acc[3 * bb + 1][2 * c2 + e])));
          u32x2 a = pk4(acc[3 * bb + 2][2 * c2]), bq = pk4(acc[3 * bb + 2][2 * c2 + 1]);
          auto r0 = __builtin_amdgcn_permlane16_swap(a[0], bq[0], false, false);
          auto r1 = __builtin_amdgcn_permlane16_swap(a[1], bq[1], false, false);
          u32x4 t = {(unsigned)r0[0], (unsigned)r1[0], (unsigned)r0[1], (unsigned)r1[1]};
          st16_wt(vb_base + (size_t)bb * 8 * 28 * 1024 + c2 * 1024, t);
        }
    }
  }
}

__global__ __launch_bounds__(512, 2) void k2_out(const char* __restrict__ ws, uint16_t* __restrict__ out) {
  __shared__ __attribute__((aligned(16))) char lds[8 * 6 * 1024];
  const int bid = blockIdx.x;
  const int rest = bid >> 3;
  const int b = (bid & 7) + 8 * (rest / 14);
  const int t = rest % 14;
  const int m = t >> 1, cp = t & 1;
  const int wave = __builtin_amdgcn_readfirstlane(threadIdx.x >> 6);
  const int lane = threadIdx.x & 63;

  u32x4 pf[3][3], wf[3][2];
#pragma unroll
  for (int cc = 0; cc < 2; ++cc) {
    const int c2 = 2 * cp + cc;
    wf[0][cc] = *(const u32x4*)(ws + WS_VA + (size_t)(((b * HQ + 2 * wave) * 7 + m) * 4 + c2) * 1024 + lane * 16);
    wf[1][cc] = *(const u32x4*)(ws + WS_VA + (size_t)(((b * HQ + 2 * wave + 1) * 7 + m) * 4 + c2) * 1024 + lane * 16);
    wf[2][cc] = *(const u32x4*)(ws + WS_VB + (size_t)(((b * 8 + wave) * 7 + m) * 4 + c2) * 1024 + lane * 16);
  }
#pragma unroll
  for (int qt = 0; qt < 3; ++qt) {
    pf[0][qt] = *(const u32x4*)(ws + WS_PA + (size_t)((b * HQ + 2 * wave) * 3 + qt) * 1024 + lane * 16);
    pf[1][qt] = *(const u32x4*)(ws + WS_PA + (size_t)((b * HQ + 2 * wave + 1) * 3 + qt) * 1024 + lane * 16);
    pf[2][qt] = *(const u32x4*)(ws + WS_PB + (size_t)((b * 8 + wave) * 3 + qt) * 1024 + lane * 16);
  }
  f32x4 acc[3][2];
#pragma unroll
  for (int qt = 0; qt < 3; ++qt)
#pragma unroll
    for (int cc = 0; cc < 2; ++cc) {
      f32x4 a = f32x4{0.f, 0.f, 0.f, 0.f};
      a = MFMA(pf[0][qt], wf[0][cc], a);
      a = MFMA(pf[1][qt], wf[1][cc], a);
      a = MFMA(pf[2][qt], wf[2][cc], a);
      acc[qt][cc] = a;
    }
#pragma unroll
  for (int qt = 0; qt < 3; ++qt)
#pragma unroll
    for (int cc = 0; cc < 2; ++cc) *(f32x4*)(lds + ((wave * 3 + qt) * 2 + cc) * 1024 + lane * 16) = acc[qt][cc];
  __syncthreads();
  const int tid = threadIdx.x;
  if (tid < 384) {
    const int q = tid >> 3, ip = tid & 7;
    const int qt = q >> 4, gq = (q >> 2) & 3, r = q & 3;
    float v[4] = {0.f, 0.f, 0.f, 0.f};
#pragma unroll
    for (int ww = 0; ww < 8; ++ww)
#pragma unroll
      for (int cc = 0; cc < 2; ++cc)
#pragma unroll
        for (int e = 0; e < 2; ++e)
          v[2 * cc + e] += *(const float*)(lds + ((ww * 3 + qt) * 2 + cc) * 1024 + ((gq << 4) | (e << 3) | ip) * 16 + r * 4);
    u32x2 o = {pk2(v[0], v[1]), pk2(v[2], v[3])};
    *(u32x2*)(out + (size_t)(b * SQ + q) * DM + 64 * m + 8 * ip + 4 * cp) = o;
  }
}

extern "C" void kernel_launch(void* const* d_in, const int* in_sizes, int n_in, void* d_out, int out_size, void* d_ws,
                              size_t ws_size, hipStream_t stream) {
  const uint16_t* x = (const uint16_t*)d_in[0];
  const uint16_t* kc = (const uint16_t*)d_in[1];
  const uint16_t* vc = (const uint16_t*)d_in[2];
  const uint16_t* wq = (const uint16_t*)d_in[3];
  const uint16_t* wo = (const uint16_t*)d_in[4];
  k1_attn<<<256, 256, 0, stream>>>(x, kc, vc, wq, wo, (char*)d_ws);
  k2_out<<<224, 512, 0, stream>>>((const char*)d_ws, (uint16_t*)d_out);
}
```

```cpp
#include <hip/hip_runtime.h>
#include <hip/hip_bf16.h>
#include <stdint.h>

typedef __attribute__((ext_vector_type(8))) __bf16 bf16x8;
typedef __attribute__((ext_vector_type(2))) __bf16 bf16x2;
typedef __attribute__((ext_vector_type(4))) float f32x4;
typedef __attribute__((ext_vector_type(2))) float f32x2;
typedef __attribute__((ext_vector_type(4))) unsigned u32x4;
typedef __attribute__((ext_vector_type(2))) unsigned u32x2;

#define DI __device__ __forceinline__
#define MFMA(a, b, c) __builtin_amdgcn_mfma_f32_16x16x32_bf16(__builtin_bit_cast(bf16x8, (a)), __builtin_bit_cast(bf16x8, (b)), (c), 0, 0, 0)
#define SB() __builtin_amdgcn_sched_barrier(0)

constexpr int NB = 16, SQ = 48, DM = 448, HQ = 16, HKV = 8, SK = 48, DH = 256;

constexpr size_t WS_PA = 0;
constexpr size_t WS_PB = WS_PA + (size_t)NB * HQ * 3 * 1024;
constexpr size_t WS_VA = WS_PB + (size_t)NB * 8 * 3 * 1024;
constexpr size_t WS_VB = WS_VA + (size_t)NB * HQ * 28 * 1024;

DI unsigned pk2(float a, float b) {
  f32x2 v = {a, b};
  bf16x2 r = __builtin_convertvector(v, bf16x2);
  return __builtin_bit_cast(unsigned, r);
}
DI u32x2 pk4(f32x4 v) {
  u32x2 r = {pk2(v[0], v[1]), pk2(v[2], v[3])};
  return r;
}
DI u32x4 cat(u32x2 a, u32x2 b) {
  u32x4 r = {a[0], a[1], b[0], b[1]};
  return r;
}
DI int swap02(int i) { return (i & 10) | ((i & 1) << 2) | ((i >> 2) & 1); }
DI void st16_wt(char* p, u32x4 v) { asm volatile("global_store_dwordx4 %0, %1, off sc1\n\ts_nop 1" ::"v"(p), "v"(v) : "memory"); }
DI void st8_wt(char* p, u32x2 v) { asm volatile("global_store_dwordx2 %0, %1, off sc1\n\ts_nop 1" ::"v"(p), "v"(v) : "memory"); }

DI float red_max(float m) {
  unsigned u = __builtin_bit_cast(unsigned, m);
  auto r = __builtin_amdgcn_permlane32_swap(u, u, false, false);
  m = fmaxf(__builtin_bit_cast(float, (unsigned)r[0]), __builtin_bit_cast(float, (unsigned)r[1]));
  u = __builtin_bit_cast(unsigned, m);
  auto r2 = __builtin_amdgcn_permlane16_swap(u, u, false, false);
  return fmaxf(__builtin_bit_cast(float, (unsigned)r2[0]), __builtin_bit_cast(float, (unsigned)r2[1]));
}
DI float red_sum(float m) {
  unsigned u = __builtin_bit_cast(unsigned, m);
  auto r = __builtin_amdgcn_permlane32_swap(u, u, false, false);
  m = __builtin_bit_cast(float, (unsigned)r[0]) + __builtin_bit_cast(float, (unsigned)r[1]);
  u = __builtin_bit_cast(unsigned, m);
  auto r2 = __builtin_amdgcn_permlane16_swap(u, u, false, false);
  return __builtin_bit_cast(float, (unsigned)r2[0]) + __builtin_bit_cast(float, (unsigned)r2[1]);
}

DI void transpose8(const u32x4 (&R)[8], u32x4 (&F)[8]) {
#pragma unroll
  for (int t = 0; t < 4; ++t)
#pragma unroll
    for (int jj = 0; jj < 4; ++jj) {
      F[2 * t][jj] = __builtin_amdgcn_perm(R[2 * jj + 1][t], R[2 * jj][t], 0x05040100u);
      F[2 * t + 1][jj] = __builtin_amdgcn_perm(R[2 * jj + 1][t], R[2 * jj][t], 0x07060302u);
    }
}

constexpr bool ROT = true;
constexpr int LDS_X = 0;
constexpr int LDS_K = 96 * 896;
constexpr int LDS_V = 0;
constexpr int LDS_TOTAL = LDS_K + 96 * 512;

DI void gld16(u32x4& dst, const char* sbase, unsigned voff) {
  asm volatile("global_load_dwordx4 %0, %1, %2 nt" : "=v"(dst) : "v"(voff), "s"(sbase) : "memory");
}
DI void dma16(unsigned lds_dst, const char* sbase, unsigned voff) {
  unsigned keep;
  asm volatile("s_mov_b32 %0, m0\n\ts_mov_b32 m0, %3\n\ts_nop 0\n\tglobal_load_lds_dwordx4 %1, %2\n\ts_mov_b32 m0, %0"
               : "=&s"(keep) : "v"(voff), "s"(sbase), "s"(lds_dst) : "memory");
}
DI void dma16nt(unsigned lds_dst, const char* sbase, unsigned voff) {
  unsigned keep;
  asm volatile("s_mov_b32 %0, m0\n\ts_mov_b32 m0, %3\n\ts_nop 0\n\tglobal_load_lds_dwordx4 %1, %2 nt\n\ts_mov_b32 m0, %0"
               : "=&s"(keep) : "v"(voff), "s"(sbase), "s"(lds_dst) : "memory");
}
template <int N>
DI void wait_vm() { asm volatile("s_waitcnt vmcnt(%0)" ::"i"(N) : "memory"); }
template <int N>
DI void wait_vm8(u32x4 (&R)[8]) {
  asm volatile("s_waitcnt vmcnt(%8)"
               : "+v"(R[0]), "+v"(R[1]), "+v"(R[2]), "+v"(R[3]), "+v"(R[4]), "+v"(R[5]), "+v"(R[6]), "+v"(R[7])
               : "i"(N) : "memory");
}

template <int OFF>
DI void gld16o(u32x4& dst, const char* sbase, unsigned voff) {
  asm volatile("global_load_dwordx4 %0, %1, %2 offset:%3 nt" : "=v"(dst) : "v"(voff), "s"(sbase), "i"(OFF) : "memory");
}
DI void transpose_q(const u32x4 (&R)[8], u32x4 (&F)[8], int t) {
#pragma unroll
  for (int jj = 0; jj < 4; ++jj) {
    F[2 * t][jj] = __builtin_amdgcn_perm(R[2 * jj + 1][t], R[2 * jj][t], 0x05040100u);
    F[2 * t + 1][jj] = __builtin_amdgcn_perm(R[2 * jj + 1][t], R[2 * jj][t], 0x07060302u);
  }
}

DI void touch(unsigned& dst, const char* p) { asm volatile("global_load_dword %0, %1, off" : "+v"(dst) : "v"(p) : "memory"); }
__global__ __launch_bounds__(256, 1) void k1_attn(const uint16_t* __restrict__ x, const uint16_t* __restrict__ kc,
                                                  const uint16_t* __restrict__ vc, const uint16_t* __restrict__ wq,
                                                  const uint16_t* __restrict__ wo, char* __restrict__ ws) {
  __shared__ __attribute__((aligned(16))) char lds[LDS_TOTAL];
  const int bid = blockIdx.x;
  const int g = bid & 7;
  const int idx = bid >> 3;
  const int p = (idx & 15) >> 1;
  const int wave = __builtin_amdgcn_readfirstlane(threadIdx.x >> 6);
  const int lane = threadIdx.x & 63;
  const int i = lane & 15, gq = lane >> 4;
  const int l8 = lane >> 3, e8 = lane & 7;
  const unsigned lds0 = (unsigned)(size_t)lds;
  if (idx < 16) {
    const int h = 2 * g + (idx & 1);
    const int half = wave & 1, kh = wave >> 1;
    auto gline = [&](int slot) {
      int r = slot + (ROT ? p : 0);
      r = r >= 7 ? r - 7 : r;
      r = r >= 7 ? r - 7 : r;
      return r;
    };
    auto line_of = [&](int t) { return 3 * kh + t; };
    auto ks_of = [&](int s) { return s < 6 ? 2 * gline(line_of(s >> 1)) + (s & 1) : 2 * gline(6) + kh; };
    const char* xs = (const char*)(x + (size_t)(2 * p) * SQ * DM);
    const unsigned xoff0 = (unsigned)(l8 * 896 + ((e8 ^ (l8 >> 1)) * 16));
    const unsigned xoff1 = (unsigned)(l8 * 896 + ((e8 ^ ((l8 >> 1) | 4)) * 16));
    auto xdma = [&](int L, int rg) { dma16(lds0 + LDS_X + L * 12288 + rg * 1024, xs + (size_t)rg * 8 * 896 + gline(L) * 128, (rg & 1) ? xoff1 : xoff0); };
    const char* kbase = (const char*)(kc + (size_t)((2 * p + (wave >> 1)) * HKV + g) * SK * DH) + (wave & 1) * 12288;
    auto kdma = [&](int n) {
      const int row = (wave * 12 + n) * 2 + (lane >> 5);
      dma16nt(lds0 + LDS_K + (wave * 12 + n) * 1024, kbase + n * 1024, (unsigned)((lane >> 5) * 512 + (((lane & 31) ^ swap02(row & 15)) * 16)));
    };
    unsigned woff[8];
#pragma unroll
    for (int j = 0; j < 8; ++j) woff[j] = (unsigned)((8 * gq + j) * (HQ * DH) + 8 * i) * 2u;
    const char* wbase = (const char*)(wq + h * DH + 128 * half);
    u32x4 R[3][8];
    auto wld = [&](int s, int j) { gld16(R[s % 3][j], wbase + (size_t)(32 * ks_of(s)) * (HQ * DH * 2), woff[j]); };
    auto xaddr = [&](int s, int qt) {
      const int L = s < 6 ? line_of(s >> 1) : 6;
      const int eo = s < 6 ? 4 * (s & 1) : 4 * kh;
      const int row = 16 * qt + i;
      return lds + LDS_X + L * 12288 + row * 128 + (((eo + gq) ^ ((row >> 1) & 7)) * 16);
    };
#pragma unroll
    for (int n = 0; n < 6; ++n) xdma(line_of(0), 6 * half + n);
#pragma unroll
    for (int j = 0; j < 8; ++j) wld(0, j);
    wait_vm<8>();
    __syncthreads();
#pragma unroll
    for (int j = 0; j < 8; ++j) wld(1, j);

    f32x4 acc[8][6];
#pragma unroll
    for (int c = 0; c < 8; ++c)
#pragma unroll
      for (int qt = 0; qt < 6; ++qt) acc[c][qt] = f32x4{0.f, 0.f, 0.f, 0.f};
    u32x4 F[2][8];
    u32x4 xb[2];
    xb[0] = *(const u32x4*)xaddr(0, 0);
    wait_vm8<8>(R[0]);
    transpose8(R[0], F[0]);
    SB();
#pragma unroll
    for (int s = 0; s < 7; ++s) {
#pragma unroll
      for (int qt = 0; qt < 6; ++qt) {
        const bool bar_after = (qt == 5) && (s == 1 || s == 3 || s == 5);
        if (qt < 5) xb[(qt + 1) & 1] = *(const u32x4*)xaddr(s, qt + 1);
        else if (s < 6 && !bar_after) xb[0] = *(const u32x4*)xaddr(s + 1, 0);
        if (qt == 2 && s < 6) {
          if (s == 0) wait_vm8<4>(R[1]);
          if (s == 1) wait_vm8<10>(R[2]);
          if (s == 2) wait_vm8<10>(R[0]);
          if (s == 3) wait_vm8<10>(R[1]);
          if (s == 4) wait_vm8<13>(R[2]);
          if (s == 5) wait_vm8<0>(R[0]);
        }
        if (qt == 0 && s == 4) { kdma(9); kdma(10); kdma(11); }
        if (qt < 4 && s < 5) { wld(s + 2, 2 * qt); wld(s + 2, 2 * qt + 1); }
        if (qt >= 4 && s == 0) { for (int n = 0; n < 3; ++n) xdma(line_of(1), 6 * half + 3 * (qt - 4) + n); }
        if (qt >= 4 && s == 1) { for (int n = 0; n < 3; ++n) xdma(line_of(2), 6 * half + 3 * (qt - 4) + n); }
        if (qt == 4 && s == 2) { for (int n = 0; n < 3; ++n) xdma(6, 3 * wave + n); }
        if (qt == 5 && s == 2) { kdma(0); kdma(1); kdma(2); }
        if (qt >= 4 && s == 3) { kdma(3 + 3 * (qt - 4)); kdma(4 + 3 * (qt - 4)); kdma(5 + 3 * (qt - 4)); }
        if (qt >= 2 && s < 6) transpose_q(R[(s + 1) % 3], F[(s + 1) & 1], qt - 2);
#pragma unroll
        for (int c = 0; c < 8; ++c) acc[c][qt] = MFMA(F[s & 1][c], xb[qt & 1], acc[c][qt]);
        if (!(s == 6)) SB();
        if (bar_after) {
          if (s == 1) wait_vm<14>();
          __syncthreads();
          xb[0] = *(const u32x4*)xaddr(s + 1, 0);
        }
      }
    }
#pragma unroll
    for (int bb = 0; bb < 2; ++bb) {
      u32x4 kf[3][4];
#pragma unroll
      for (int kt = 0; kt < 3; ++kt)
#pragma unroll
        for (int s = 0; s < 4; ++s) {
          const int row = 48 * bb + 16 * kt + i;
          kf[kt][s] = *(const u32x4*)(lds + LDS_K + row * 512 + (((16 * half + 4 * gq + s) ^ swap02(i)) * 16));
        }
#pragma unroll
      for (int qt = 0; qt < 3; ++qt) {
        const int q6 = 3 * bb + qt;
        f32x4 sacc[3];
#pragma unroll
        for (int kt = 0; kt < 3; ++kt) sacc[kt] = f32x4{0.f, 0.f, 0.f, 0.f};
#pragma unroll
        for (int s = 0; s < 4; ++s) {
          u32x4 qf = {pk2(acc[0][q6][s], acc[1][q6][s]), pk2(acc[2][q6][s], acc[3][q6][s]),
                      pk2(acc[4][q6][s], acc[5][q6][s]), pk2(acc[6][q6][s], acc[7][q6][s])};
#pragma unroll
          for (int kt = 0; kt < 3; ++kt) sacc[kt] = MFMA(kf[kt][s], qf, sacc[kt]);
        }
#pragma unroll
        for (int kt = 0; kt < 3; ++kt)
          *(f32x4*)(lds + LDS_X + (wave * 18 + (bb * 3 + qt) * 3 + kt) * 1024 + lane * 16) = sacc[kt];
      }
    }
    __syncthreads();
    {
      f32x4 s[2][3];
      const int u1 = wave < 2 ? wave + 4 : wave;
#pragma unroll
      for (int rep = 0; rep < 2; ++rep) {
        const int u = rep ? u1 : wave;
#pragma unroll
        for (int kt = 0; kt < 3; ++kt) {
          s[rep][kt] = *(const f32x4*)(lds + LDS_X + (0 * 18 + u * 3 + kt) * 1024 + lane * 16);
#pragma unroll
          for (int ww = 1; ww < 4; ++ww) s[rep][kt] += *(const f32x4*)(lds + LDS_X + (ww * 18 + u * 3 + kt) * 1024 + lane * 16);
        }
      }
      const float sc = 0.0625f * 1.44269504088896340736f;
#pragma unroll
      for (int rep = 0; rep < 2; ++rep) {
        float m = -INFINITY;
#pragma unroll
        for (int kt = 0; kt < 3; ++kt)
#pragma unroll
          for (int r = 0; r < 4; ++r) m = fmaxf(m, s[rep][kt][r]);
        m = red_max(m) * sc;
        float sum = 0.f;
#pragma unroll
        for (int kt = 0; kt < 3; ++kt)
#pragma unroll
          for (int r = 0; r < 4; ++r) {
            s[rep][kt][r] = __builtin_amdgcn_exp2f(__builtin_fmaf(s[rep][kt][r], sc, -m));
            sum += s[rep][kt][r];
          }
        sum = red_sum(sum);
        const float inv = 1.0f / sum;
#pragma unroll
        for (int kt = 0; kt < 3; ++kt) s[rep][kt] *= inv;
      }
#pragma unroll
      for (int rep = 0; rep < 2; ++rep) {
        if (rep == 0 || wave < 2) {
          const int u = rep ? u1 : wave;
          const int bb = u >= 3 ? 1 : 0, qt = u - 3 * bb;
          const int b = 2 * p + bb;
          st16_wt(ws + WS_PA + (size_t)((b * HQ + h) * 3 + qt) * 1024 + lane * 16, cat(pk4(s[rep][0]), pk4(s[rep][1])));
          st8_wt(ws + WS_PB + (size_t)((b * 8 + g) * 3 + qt) * 1024 + (((2 * (h & 1) + (gq >> 1)) << 4) | i) * 16 + (gq & 1) * 8, pk4(s[rep][2]));
        }
      }
    }
  } else {
    const int j = 4 * (idx & 1) + wave;
    const bool active = j < 7;
    auto line_of = [&](int t) { return ROT ? (t + p) & 3 : t; };
    const unsigned voff0 = (unsigned)(l8 * 512 + ((e8 ^ (l8 >> 1)) * 16));
    const unsigned voff1 = (unsigned)(l8 * 512 + ((e8 ^ ((l8 >> 1) | 4)) * 16));
    auto vdma = [&](int d, int n) {
      const int q = 3 * wave + n;
      const int t = 2 * (d & 1) + (q >= 6 ? 1 : 0);
      const int rgl = q >= 6 ? q - 6 : q;
      const int bb = d >> 1;
      const int L = line_of(t);
      const char* vs_ = (const char*)(vc + (size_t)((2 * p + bb) * HKV + g) * SK * DH);
      dma16nt(lds0 + LDS_V + L * 12288 + (6 * bb + rgl) * 1024, vs_ + (size_t)rgl * 8 * 512 + L * 128, (rgl & 1) ? voff1 : voff0);
    };
    if (!active) {
#pragma unroll
      for (int d = 0; d < 4; ++d)
#pragma unroll
        for (int n = 0; n < 3; ++n) vdma(d, n);
      wait_vm<9>();
      __syncthreads();
      wait_vm<6>();
      __syncthreads();
      wait_vm<0>();
      __syncthreads();
    } else {
      const int np = 128 * j + 8 * i;
      const int hl = np >= DM ? 1 : 0;
      const int n0 = np - DM * hl;
      const unsigned woffa = (unsigned)((hl * DH + 8 * gq) * DM + n0) * 2u;
      const unsigned woffb = woffa + 4 * DM * 2;
      const char* wbase = (const char*)(wo + (size_t)(2 * g) * DH * DM);
      u32x4 R[3][8];
      auto wld = [&](int s, int jr) {
        const char* sb = wbase + (size_t)(32 * (2 * line_of(s >> 1) + (s & 1))) * (DM * 2);
        if (jr == 0) gld16o<0>(R[s % 3][0], sb, woffa);
        if (jr == 1) gld16o<896>(R[s % 3][1], sb, woffa);
        if (jr == 2) gld16o<1792>(R[s % 3][2], sb, woffa);
        if (jr == 3) gld16o<2688>(R[s % 3][3], sb, woffa);
        if (jr == 4) gld16o<0>(R[s % 3][4], sb, woffb);
        if (jr == 5) gld16o<896>(R[s % 3][5], sb, woffb);
        if (jr == 6) gld16o<1792>(R[s % 3][6], sb, woffb);
        if (jr == 7) gld16o<2688>(R[s % 3][7], sb, woffb);
      };
      auto vaddr = [&](int s, int mt) {
        const int row = 16 * mt + i;
        return lds + LDS_V + line_of(s >> 1) * 12288 + row * 128 + (((4 * (s & 1) + gq) ^ ((row >> 1) & 7)) * 16);
      };
      const int m = n0 >> 6, ip = (n0 >> 3) & 7;
      const int h = 2 * g + hl;
      char* va_base = ws + WS_VA + (size_t)(((2 * p * HQ + h) * 7 + m) * 4) * 1024 + ((gq << 4) | ip) * 16;
      char* vb_base = ws + WS_VB + (size_t)(((2 * p * 8 + g) * 7 + m) * 4) * 1024 + (((2 * hl + (gq >> 1)) << 4) | ((gq & 1) << 3) | ip) * 16;
      auto store_c2 = [&](f32x4 (&acc)[3][8], int bb, int c2) {
#pragma unroll
        for (int e = 0; e < 2; ++e)
          st16_wt(va_base + (size_t)bb * HQ * 28 * 1024 + c2 * 1024 + e * 128, cat(pk4(acc[0][2 * c2 + e]), pk4(acc[1][2 * c2 + e])));
        u32x2 a = pk4(acc[2][2 * c2]), bq = pk4(acc[2][2 * c2 + 1]);
        auto r0 = __builtin_amdgcn_permlane16_swap(a[0], bq[0], false, false);
        auto r1 = __builtin_amdgcn_permlane16_swap(a[1], bq[1], false, false);
        u32x4 t = {(unsigned)r0[0], (unsigned)r1[0], (unsigned)r0[1], (unsigned)r1[1]};
        st16_wt(vb_base + (size_t)bb * 8 * 28 * 1024 + c2 * 1024, t);
      };
#pragma unroll
      for (int n = 0; n < 3; ++n) vdma(0, n);
#pragma unroll
      for (int jr = 0; jr < 8; ++jr) wld(0, jr);
      wait_vm<8>();
      __syncthreads();
#pragma unroll
      for (int jr = 0; jr < 8; ++jr) wld(1, jr);
      f32x4 acc0[3][8], acc1[3][8];
#pragma unroll
      for (int mt = 0; mt < 3; ++mt)
#pragma unroll
        for (int c = 0; c < 8; ++c) { acc0[mt][c] = f32x4{0.f, 0.f, 0.f, 0.f}; acc1[mt][c] = f32x4{0.f, 0.f, 0.f, 0.f}; }
      u32x4 F[8][8];
      u32x4 va[2];
      va[0] = *(const u32x4*)vaddr(0, 0);
      wait_vm8<8>(R[0]);
      transpose8(R[0], F[0]);
      SB();
#pragma unroll
      for (int s = 0; s < 8; ++s) {
#pragma unroll
        for (int mt = 0; mt < 3; ++mt) {
          const int gidx = 3 * s + mt;
          const bool bar_after = (mt == 2) && (s == 3 || s == 7);
          if (mt < 2) va[(gidx + 1) & 1] = *(const u32x4*)vaddr(s, mt + 1);
          else if (!bar_after) va[(gidx + 1) & 1] = *(const u32x4*)vaddr(s + 1, 0);
          if (mt == 1 && s < 7) {
            if (s == 0) wait_vm8<3>(R[1]);
            if (s == 1) wait_vm8<6>(R[2]);
            if (s == 2) wait_vm8<6>(R[0]);
            if (s == 3) wait_vm8<6>(R[1]);
            if (s == 4) wait_vm8<3>(R[2]);
            if (s == 5) wait_vm8<3>(R[0]);
            if (s == 6) wait_vm8<0>(R[1]);
          }
          if (s < 6) {
            if (mt == 0) { wld(s + 2, 0); wld(s + 2, 1); wld(s + 2, 2); }
            if (mt == 1) { wld(s + 2, 3); wld(s + 2, 4); wld(s + 2, 5); }
            if (mt == 2) { wld(s + 2, 6); wld(s + 2, 7); }
          }
          if (mt == 2 && s < 3) { for (int n = 0; n < 3; ++n) vdma(s + 1, n); }
          if (mt >= 1 && s < 7) { transpose_q(R[(s + 1) % 3], F[s + 1], 2 * (mt - 1)); transpose_q(R[(s + 1) % 3], F[s + 1], 2 * (mt - 1) + 1); }
#pragma unroll
          for (int c = 0; c < 8; ++c) acc0[mt][c] = MFMA(va[gidx & 1], F[s][c], acc0[mt][c]);
          SB();
          if (bar_after) {
            __syncthreads();
            va[(gidx + 1) & 1] = *(const u32x4*)(s == 7 ? vaddr(0, 3) : vaddr(s + 1, 0));
          }
        }
      }
#pragma unroll
      for (int s = 0; s < 8; ++s) {
#pragma unroll
        for (int mt = 0; mt < 3; ++mt) {
          const int gidx = 3 * s + mt;
          if (mt < 2) va[(gidx + 1) & 1] = *(const u32x4*)vaddr(s, 3 + mt + 1);
          else if (s < 7) va[(gidx + 1) & 1] = *(const u32x4*)vaddr(s + 1, 3);
          if (gidx < 4) store_c2(acc0, 0, gidx);
#pragma unroll
          for (int c = 0; c < 8; ++c) acc1[mt][c] = MFMA(va[gidx & 1], F[s][c], acc1[mt][c]);
          if (s < 7) SB();
        }
      }
#pragma unroll
      for (int c2 = 0; c2 < 4; ++c2) store_c2(acc1, 1, c2);
    }
  }
}

__global__ __launch_bounds__(512, 2) void k2_out(const char* __restrict__ ws, uint16_t* __restrict__ out) {
  __shared__ __attribute__((aligned(16))) char lds[8 * 6 * 1024];
  const int bid = blockIdx.x;
  const int rest = bid >> 3;
  const int b = (bid & 7) + 8 * (rest / 14);
  const int t = rest % 14;
  const int m = t >> 1, cp = t & 1;
  const int wave = __builtin_amdgcn_readfirstlane(threadIdx.x >> 6);
  const int lane = threadIdx.x & 63;

  u32x4 pf[3][3], wf[3][2];
#pragma unroll
  for (int cc = 0; cc < 2; ++cc) {
    const int c2 = 2 * cp + cc;
    wf[0][cc] = *(const u32x4*)(ws + WS_VA + (size_t)(((b * HQ + 2 * wave) * 7 + m) * 4 + c2) * 1024 + lane * 16);
    wf[1][cc] = *(const u32x4*)(ws + WS_VA + (size_t)(((b * HQ + 2 * wave + 1) * 7 + m) * 4 + c2) * 1024 + lane * 16);
    wf[2][cc] = *(const u32x4*)(ws + WS_VB + (size_t)(((b * 8 + wave) * 7 + m) * 4 + c2) * 1024 + lane * 16);
  }
#pragma unroll
  for (int qt = 0; qt < 3; ++qt) {
    pf[0][qt] = *(const u32x4*)(ws + WS_PA + (size_t)((b * HQ + 2 * wave) * 3 + qt) * 1024 + lane * 16);
    pf[1][qt] = *(const u32x4*)(ws + WS_PA + (size_t)((b * HQ + 2 * wave + 1) * 3 + qt) * 1024 + lane * 16);
    pf[2][qt] = *(const u32x4*)(ws + WS_PB + (size_t)((b * 8 + wave) * 3 + qt) * 1024 + lane * 16);
  }
  f32x4 acc[3][2];
#pragma unroll
  for (int qt = 0; qt < 3; ++qt)
#pragma unroll
    for (int cc = 0; cc < 2; ++cc) {
      f32x4 a = f32x4{0.f, 0.f, 0.f, 0.f};
      a = MFMA(pf[0][qt], wf[0][cc], a);
      a = MFMA(pf[1][qt], wf[1][cc], a);
      a = MFMA(pf[2][qt], wf[2][cc], a);
      acc[qt][cc] = a;
    }
#pragma unroll
  for (int qt = 0; qt < 3; ++qt)
#pragma unroll
    for (int cc = 0; cc < 2; ++cc) *(f32x4*)(lds + ((wave * 3 + qt) * 2 + cc) * 1024 + lane * 16) = acc[qt][cc];
  __syncthreads();
  const int tid = threadIdx.x;
  if (tid < 384) {
    const int q = tid >> 3, ip = tid & 7;
    const int qt = q >> 4, gq = (q >> 2) & 3, r = q & 3;
    float v[4] = {0.f, 0.f, 0.f, 0.f};
#pragma unroll
    for (int ww = 0; ww < 8; ++ww)
#pragma unroll
      for (int cc = 0; cc < 2; ++cc)
#pragma unroll
        for (int e = 0; e < 2; ++e)
          v[2 * cc + e] += *(const float*)(lds + ((ww * 3 + qt) * 2 + cc) * 1024 + ((gq << 4) | (e << 3) | ip) * 16 + r * 4);
    u32x2 o = {pk2(v[0], v[1]), pk2(v[2], v[3])};
    *(u32x2*)(out + (size_t)(b * SQ + q) * DM + 64 * m + 8 * ip + 4 * cp) = o;
  }
}

extern "C" void kernel_launch(void* const* d_in, const int* in_sizes, int n_in, void* d_out, int out_size, void* d_ws,
                              size_t ws_size, hipStream_t stream) {
  const uint16_t* x = (const uint16_t*)d_in[0];
  const uint16_t* kc = (const uint16_t*)d_in[1];
  const uint16_t* vc = (const uint16_t*)d_in[2];
  const uint16_t* wq = (const uint16_t*)d_in[3];
  const uint16_t* wo = (const uint16_t*)d_in[4];
  k1_attn<<<256, 256, 0, stream>>>(x, kc, vc, wq, wo, (char*)d_ws);
  k2_out<<<224, 512, 0, stream>>>((const char*)d_ws, (uint16_t*)d_out);
}
```

```cpp
#include <hip/hip_runtime.h>
#include <hip/hip_bf16.h>
#include <stdint.h>

typedef __attribute__((ext_vector_type(8))) __bf16 bf16x8;
typedef __attribute__((ext_vector_type(2))) __bf16 bf16x2;
typedef __attribute__((ext_vector_type(4))) float f32x4;
typedef __attribute__((ext_vector_type(2))) float f32x2;
typedef __attribute__((ext_vector_type(4))) unsigned u32x4;
typedef __attribute__((ext_vector_type(2))) unsigned u32x2;

#define DI __device__ __forceinline__
#define MFMA(a, b, c) __builtin_amdgcn_mfma_f32_16x16x32_bf16(__builtin_bit_cast(bf16x8, (a)), __builtin_bit_cast(bf16x8, (b)), (c), 0, 0, 0)
#define SB() __builtin_amdgcn_sched_barrier(0)
#define NTS ""

constexpr int NB = 16, SQ = 48, DM = 448, HQ = 16, HKV = 8, SK = 48, DH = 256;

constexpr size_t WS_PA = 0;
constexpr size_t WS_PB = WS_PA + (size_t)NB * HQ * 3 * 1024;
constexpr size_t WS_VA = WS_PB + (size_t)NB * 8 * 3 * 1024;
constexpr size_t WS_VB = WS_VA + (size_t)NB * HQ * 28 * 1024;
constexpr size_t WS_FL = WS_VB + (size_t)NB * 8 * 28 * 1024;
constexpr unsigned long long MAGIC = 0x9E3779B97F4A7C15ull;

DI unsigned pk2(float a, float b) {
  f32x2 v = {a, b};
  bf16x2 r = __builtin_convertvector(v, bf16x2);
  return __builtin_bit_cast(unsigned, r);
}
DI u32x2 pk4(f32x4 v) {
  u32x2 r = {pk2(v[0], v[1]), pk2(v[2], v[3])};
  return r;
}
DI u32x4 cat(u32x2 a, u32x2 b) {
  u32x4 r = {a[0], a[1], b[0], b[1]};
  return r;
}
DI int swap02(int i) { return (i & 10) | ((i & 1) << 2) | ((i >> 2) & 1); }
DI void st16_wt(char* p, u32x4 v) { asm volatile("global_store_dwordx4 %0, %1, off sc1\n\ts_nop 1" ::"v"(p), "v"(v) : "memory"); }
DI void st8_wt(char* p, u32x2 v) { asm volatile("global_store_dwordx2 %0, %1, off sc1\n\ts_nop 1" ::"v"(p), "v"(v) : "memory"); }

DI float red_max(float m) {
  unsigned u = __builtin_bit_cast(unsigned, m);
  auto r = __builtin_amdgcn_permlane32_swap(u, u, false, false);
  m = fmaxf(__builtin_bit_cast(float, (unsigned)r[0]), __builtin_bit_cast(float, (unsigned)r[1]));
  u = __builtin_bit_cast(unsigned, m);
  auto r2 = __builtin_amdgcn_permlane16_swap(u, u, false, false);
  return fmaxf(__builtin_bit_cast(float, (unsigned)r2[0]), __builtin_bit_cast(float, (unsigned)r2[1]));
}
DI float red_sum(float m) {
  unsigned u = __builtin_bit_cast(unsigned, m);
  auto r = __builtin_amdgcn_permlane32_swap(u, u, false, false);
  m = __builtin_bit_cast(float, (unsigned)r[0]) + __builtin_bit_cast(float, (unsigned)r[1]);
  u = __builtin_bit_cast(unsigned, m);
  auto r2 = __builtin_amdgcn_permlane16_swap(u, u, false, false);
  return __builtin_bit_cast(float, (unsigned)r2[0]) + __builtin_bit_cast(float, (unsigned)r2[1]);
}

DI void transpose8(const u32x4 (&R)[8], u32x4 (&F)[8]) {
#pragma unroll
  for (int t = 0; t < 4; ++t)
#pragma unroll
    for (int jj = 0; jj < 4; ++jj) {
      F[2 * t][jj] = __builtin_amdgcn_perm(R[2 * jj + 1][t], R[2 * jj][t], 0x05040100u);
      F[2 * t + 1][jj] = __builtin_amdgcn_perm(R[2 * jj + 1][t], R[2 * jj][t], 0x07060302u);
    }
}

constexpr bool ROT = true;
constexpr int LDS_X = 0;
constexpr int LDS_K = 96 * 896;
constexpr int LDS_V = 0;
constexpr int LDS_TOTAL = LDS_K + 96 * 512;

DI void gld16(u32x4& dst, const char* sbase, unsigned voff) {
  asm volatile("global_load_dwordx4 %0, %1, %2" NTS : "=v"(dst) : "v"(voff), "s"(sbase) : "memory");
}
DI void gld16c(u32x4& dst, const char* sbase, unsigned voff) {
  asm volatile("global_load_dwordx4 %0, %1, %2 sc1" : "=v"(dst) : "v"(voff), "s"(sbase) : "memory");
}
DI void dma16(unsigned lds_dst, const char* sbase, unsigned voff) {
  unsigned keep;
  asm volatile("s_mov_b32 %0, m0\n\ts_mov_b32 m0, %3\n\ts_nop 0\n\tglobal_load_lds_dwordx4 %1, %2\n\ts_mov_b32 m0, %0"
               : "=&s"(keep) : "v"(voff), "s"(sbase), "s"(lds_dst) : "memory");
}
DI void dma16nt(unsigned lds_dst, const char* sbase, unsigned voff) {
  unsigned keep;
  asm volatile("s_mov_b32 %0, m0\n\ts_mov_b32 m0, %3\n\ts_nop 0\n\tglobal_load_lds_dwordx4 %1, %2" NTS "\n\ts_mov_b32 m0, %0"
               : "=&s"(keep) : "v"(voff), "s"(sbase), "s"(lds_dst) : "memory");
}
template <int N>
DI void wait_vm() { asm volatile("s_waitcnt vmcnt(%0)" ::"i"(N) : "memory"); }
template <int N>
DI void wait_vm8(u32x4 (&R)[8]) {
  asm volatile("s_waitcnt vmcnt(%8)"
               : "+v"(R[0]), "+v"(R[1]), "+v"(R[2]), "+v"(R[3]), "+v"(R[4]), "+v"(R[5]), "+v"(R[6]), "+v"(R[7])
               : "i"(N) : "memory");
}

template <int OFF>
DI void gld16o(u32x4& dst, const char* sbase, unsigned voff) {
  asm volatile("global_load_dwordx4 %0, %1, %2 offset:%3" NTS : "=v"(dst) : "v"(voff), "s"(sbase), "i"(OFF) : "memory");
}
DI void transpose_q(const u32x4 (&R)[8], u32x4 (&F)[8], int t) {
#pragma unroll
  for (int jj = 0; jj < 4; ++jj) {
    F[2 * t][jj] = __builtin_amdgcn_perm(R[2 * jj + 1][t], R[2 * jj][t], 0x05040100u);
    F[2 * t + 1][jj] = __builtin_amdgcn_perm(R[2 * jj + 1][t], R[2 * jj][t], 0x07060302u);
  }
}

DI void touch(unsigned& dst, const char* p) { asm volatile("global_load_dword %0, %1, off" : "+v"(dst) : "v"(p) : "memory"); }
__global__ __launch_bounds__(256, 1) void k1_attn(const uint16_t* __restrict__ x, const uint16_t* __restrict__ kc,
                                                  const uint16_t* __restrict__ vc, const uint16_t* __restrict__ wq,
                                                  const uint16_t* __restrict__ wo, char* __restrict__ ws,
                                                  uint16_t* __restrict__ out) {
  __shared__ __attribute__((aligned(16))) char lds[LDS_TOTAL];
  const int bid = blockIdx.x;
  const int g = bid & 7;
  const int idx = bid >> 3;
  const int p = (idx & 15) >> 1;
  const int wave = __builtin_amdgcn_readfirstlane(threadIdx.x >> 6);
  const int lane = threadIdx.x & 63;
  const int i = lane & 15, gq = lane >> 4;
  const int l8 = lane >> 3, e8 = lane & 7;
  const unsigned lds0 = (unsigned)(size_t)lds;
  if (idx < 16) {
    const int h = 2 * g + (idx & 1);
    const int half = wave & 1, kh = wave >> 1;
    auto gline = [&](int slot) {
      int r = slot + (ROT ? p : 0);
      r = r >= 7 ? r - 7 : r;
      r = r >= 7 ? r - 7 : r;
      return r;
    };
    auto line_of = [&](int t) { return 3 * kh + t; };
    auto ks_of = [&](int s) { return s < 6 ? 2 * gline(line_of(s >> 1)) + (s & 1) : 2 * gline(6) + kh; };
    const char* xs = (const char*)(x + (size_t)(2 * p) * SQ * DM);
    const unsigned xoff0 = (unsigned)(l8 * 896 + ((e8 ^ (l8 >> 1)) * 16));
    const unsigned xoff1 = (unsigned)(l8 * 896 + ((e8 ^ ((l8 >> 1) | 4)) * 16));
    auto xdma = [&](int L, int rg) { dma16(lds0 + LDS_X + L * 12288 + rg * 1024, xs + (size_t)rg * 8 * 896 + gline(L) * 128, (rg & 1) ? xoff1 : xoff0); };
    const char* kbase = (const char*)(kc + (size_t)((2 * p + (wave >> 1)) * HKV + g) * SK * DH) + (wave & 1) * 12288;
    auto kdma = [&](int n) {
      const int row = (wave * 12 + n) * 2 + (lane >> 5);
      dma16nt(lds0 + LDS_K + (wave * 12 + n) * 1024, kbase + n * 1024, (unsigned)((lane >> 5) * 512 + (((lane & 31) ^ swap02(row & 15)) * 16)));
    };
    unsigned woff[8];
#pragma unroll
    for (int j = 0; j < 8; ++j) woff[j] = (unsigned)((8 * gq + j) * (HQ * DH) + 8 * i) * 2u;
    const char* wbase = (const char*)(wq + h * DH + 128 * half);
    u32x4 R[3][8];
    auto wld = [&](int s, int j) { gld16(R[s % 3][j], wbase + (size_t)(32 * ks_of(s)) * (HQ * DH * 2), woff[j]); };
    auto xaddr = [&](int s, int qt) {
      const int L = s < 6 ? line_of(s >> 1) : 6;
      const int eo = s < 6 ? 4 * (s & 1) : 4 * kh;
      const int row = 16 * qt + i;
      return lds + LDS_X + L * 12288 + row * 128 + (((eo + gq) ^ ((row >> 1) & 7)) * 16);
    };
#pragma unroll
    for (int n = 0; n < 6; ++n) xdma(line_of(0), 6 * half + n);
#pragma unroll
    for (int j = 0; j < 8; ++j) wld(0, j);
    wait_vm<8>();
    __syncthreads();
#pragma unroll
    for (int j = 0; j < 8; ++j) wld(1, j);

    f32x4 acc[8][6];
#pragma unroll
    for (int c = 0; c < 8; ++c)
#pragma unroll
      for (int qt = 0; qt < 6; ++qt) acc[c][qt] = f32x4{0.f, 0.f, 0.f, 0.f};
    u32x4 F[2][8];
    u32x4 xb[2];
    xb[0] = *(const u32x4*)xaddr(0, 0);
    wait_vm8<8>(R[0]);
    transpose8(R[0], F[0]);
    SB();
#pragma unroll
    for (int s = 0; s < 7; ++s) {
#pragma unroll
      for (int qt = 0; qt < 6; ++qt) {
        const bool bar_after = (qt == 5) && (s == 1 || s == 3 || s == 5);
        if (qt < 5) xb[(qt + 1) & 1] = *(const u32x4*)xaddr(s, qt + 1);
        else if (s < 6 && !bar_after) xb[0] = *(const u32x4*)xaddr(s + 1, 0);
        if (qt == 2 && s < 6) {
          if (s == 0) wait_vm8<4>(R[1]);
          if (s == 1) wait_vm8<10>(R[2]);
          if (s == 2) wait_vm8<10>(R[0]);
          if (s == 3) wait_vm8<10>(R[1]);
          if (s == 4) wait_vm8<13>(R[2]);
          if (s == 5) wait_vm8<0>(R[0]);
        }
        if (qt == 0 && s == 4) { kdma(9); kdma(10); kdma(11); }
        if (qt < 4 && s < 5) { wld(s + 2, 2 * qt); wld(s + 2, 2 * qt + 1); }
        if (qt >= 4 && s == 0) { for (int n = 0; n < 3; ++n) xdma(line_of(1), 6 * half + 3 * (qt - 4) + n); }
        if (qt >= 4 && s == 1) { for (int n = 0; n < 3; ++n) xdma(line_of(2), 6 * half + 3 * (qt - 4) + n); }
        if (qt == 4 && s == 2) { for (int n = 0; n < 3; ++n) xdma(6, 3 * wave + n); }
        if (qt == 5 && s == 2) { kdma(0); kdma(1); kdma(2); }
        if (qt >= 4 && s == 3) { kdma(3 + 3 * (qt - 4)); kdma(4 + 3 * (qt - 4)); kdma(5 + 3 * (qt - 4)); }
        if (qt >= 2 && s < 6) transpose_q(R[(s + 1) % 3], F[(s + 1) & 1], qt - 2);
#pragma unroll
        for (int c = 0; c < 8; ++c) acc[c][qt] = MFMA(F[s & 1][c], xb[qt & 1], acc[c][qt]);
        if (!(s == 6)) SB();
        if (bar_after) {
          if (s == 1) wait_vm<14>();
          __syncthreads();
          xb[0] = *(const u32x4*)xaddr(s + 1, 0);
        }
      }
    }
#pragma unroll
    for (int bb = 0; bb < 2; ++bb) {
      u32x4 kf[3][4];
#pragma unroll
      for (int kt = 0; kt < 3; ++kt)
#pragma unroll
        for (int s = 0; s < 4; ++s) {
          const int row = 48 * bb + 16 * kt + i;
          kf[kt][s] = *(const u32x4*)(lds + LDS_K + row * 512 + (((16 * half + 4 * gq + s) ^ swap02(i)) * 16));
        }
#pragma unroll
      for (int qt = 0; qt < 3; ++qt) {
        const int q6 = 3 * bb + qt;
        f32x4 sacc[3];
#pragma unroll
        for (int kt = 0; kt < 3; ++kt) sacc[kt] = f32x4{0.f, 0.f, 0.f, 0.f};
#pragma unroll
        for (int s = 0; s < 4; ++s) {
          u32x4 qf = {pk2(acc[0][q6][s], acc[1][q6][s]), pk2(acc[2][q6][s], acc[3][q6][s]),
                      pk2(acc[4][q6][s], acc[5][q6][s]), pk2(acc[6][q6][s], acc[7][q6][s])};
#pragma unroll
          for (int kt = 0; kt < 3; ++kt) sacc[kt] = MFMA(kf[kt][s], qf, sacc[kt]);
        }
#pragma unroll
        for (int kt = 0; kt < 3; ++kt)
          *(f32x4*)(lds + LDS_X + (wave * 18 + (bb * 3 + qt) * 3 + kt) * 1024 + lane * 16) = sacc[kt];
      }
    }
    __syncthreads();
    {
      f32x4 s[2][3];
      const int u1 = wave < 2 ? wave + 4 : wave;
#pragma unroll
      for (int rep = 0; rep < 2; ++rep) {
        const int u = rep ? u1 : wave;
#pragma unroll
        for (int kt = 0; kt < 3; ++kt) {
          s[rep][kt] = *(const f32x4*)(lds + LDS_X + (0 * 18 + u * 3 + kt) * 1024 + lane * 16);
#pragma unroll
          for (int ww = 1; ww < 4; ++ww) s[rep][kt] += *(const f32x4*)(lds + LDS_X + (ww * 18 + u * 3 + kt) * 1024 + lane * 16);
        }
      }
      const float sc = 0.0625f * 1.44269504088896340736f;
#pragma unroll
      for (int rep = 0; rep < 2; ++rep) {
        float m = -INFINITY;
#pragma unroll
        for (int kt = 0; kt < 3; ++kt)
#pragma unroll
          for (int r = 0; r < 4; ++r) m = fmaxf(m, s[rep][kt][r]);
        m = red_max(m) * sc;
        float sum = 0.f;
#pragma unroll
        for (int kt = 0; kt < 3; ++kt)
#pragma unroll
          for (int r = 0; r < 4; ++r) {
            s[rep][kt][r] = __builtin_amdgcn_exp2f(__builtin_fmaf(s[rep][kt][r], sc, -m));
            sum += s[rep][kt][r];
          }
        sum = red_sum(sum);
        const float inv = 1.0f / sum;
#pragma unroll
        for (int kt = 0; kt < 3; ++kt) s[rep][kt] *= inv;
      }
#pragma unroll
      for (int rep = 0; rep < 2; ++rep) {
        if (rep == 0 || wave < 2) {
          const int u = rep ? u1 : wave;
          const int bb = u >= 3 ? 1 : 0, qt = u - 3 * bb;
          const int b = 2 * p + bb;
          st16_wt(ws + WS_PA + (size_t)((b * HQ + h) * 3 + qt) * 1024 + lane * 16, cat(pk4(s[rep][0]), pk4(s[rep][1])));
          st8_wt(ws + WS_PB + (size_t)((b * 8 + g) * 3 + qt) * 1024 + (((2 * (h & 1) + (gq >> 1)) << 4) | i) * 16 + (gq & 1) * 8, pk4(s[rep][2]));
        }
      }
    }
  } else {
    const int j = 4 * (idx & 1) + wave;
    const bool active = j < 7;
    auto line_of = [&](int t) { return ROT ? (t + p) & 3 : t; };
    const unsigned voff0 = (unsigned)(l8 * 512 + ((e8 ^ (l8 >> 1)) * 16));
    const unsigned voff1 = (unsigned)(l8 * 512 + ((e8 ^ ((l8 >> 1) | 4)) * 16));
    auto vdma = [&](int d, int n) {
      const int q = 3 * wave + n;
      const int t = 2 * (d & 1) + (q >= 6 ? 1 : 0);
      const int rgl = q >= 6 ? q - 6 : q;
      const int bb = d >> 1;
      const int L = line_of(t);
      const char* vs_ = (const char*)(vc + (size_t)((2 * p + bb) * HKV + g) * SK * DH);
      dma16nt(lds0 + LDS_V + L * 12288 + (6 * bb + rgl) * 1024, vs_ + (size_t)rgl * 8 * 512 + L * 128, (rgl & 1) ? voff1 : voff0);
    };
    if (!active) {
#pragma unroll
      for (int d = 0; d < 4; ++d)
#pragma unroll
        for (int n = 0; n < 3; ++n) vdma(d, n);
      wait_vm<9>();
      __syncthreads();
      wait_vm<6>();
      __syncthreads();
      wait_vm<0>();
      __syncthreads();
    } else {
      const int np = 128 * j + 8 * i;
      const int hl = np >= DM ? 1 : 0;
      const int n0 = np - DM * hl;
      const unsigned woffa = (unsigned)((hl * DH + 8 * gq) * DM + n0) * 2u;
      const unsigned woffb = woffa + 4 * DM * 2;
      const char* wbase = (const char*)(wo + (size_t)(2 * g) * DH * DM);
      u32x4 R[3][8];
      auto wld = [&](int s, int jr) {
        const char* sb = wbase + (size_t)(32 * (2 * line_of(s >> 1) + (s & 1))) * (DM * 2);
        if (jr == 0) gld16o<0>(R[s % 3][0], sb, woffa);
        if (jr == 1) gld16o<896>(R[s % 3][1], sb, woffa);
        if (jr == 2) gld16o<1792>(R[s % 3][2], sb, woffa);
        if (jr == 3) gld16o<2688>(R[s % 3][3], sb, woffa);
        if (jr == 4) gld16o<0>(R[s % 3][4], sb, woffb);
        if (jr == 5) gld16o<896>(R[s % 3][5], sb, woffb);
        if (jr == 6) gld16o<1792>(R[s % 3][6], sb, woffb);
        if (jr == 7) gld16o<2688>(R[s % 3][7], sb, woffb);
      };
      auto vaddr = [&](int s, int mt) {
        const int row = 16 * mt + i;
        return lds + LDS_V + line_of(s >> 1) * 12288 + row * 128 + (((4 * (s & 1) + gq) ^ ((row >> 1) & 7)) * 16);
      };
      const int m = n0 >> 6, ip = (n0 >> 3) & 7;
      const int h = 2 * g + hl;
      char* va_base = ws + WS_VA + (size_t)(((2 * p * HQ + h) * 7 + m) * 4) * 1024 + ((gq << 4) | ip) * 16;
      char* vb_base = ws + WS_VB + (size_t)(((2 * p * 8 + g) * 7 + m) * 4) * 1024 + (((2 * hl + (gq >> 1)) << 4) | ((gq & 1) << 3) | ip) * 16;
      auto store_c2 = [&](f32x4 (&acc)[3][8], int bb, int c2) {
#pragma unroll
        for (int e = 0; e < 2; ++e)
          st16_wt(va_base + (size_t)bb * HQ * 28 * 1024 + c2 * 1024 + e * 128, cat(pk4(acc[0][2 * c2 + e]), pk4(acc[1][2 * c2 + e])));
        u32x2 a = pk4(acc[2][2 * c2]), bq = pk4(acc[2][2 * c2 + 1]);
        auto r0 = __builtin_amdgcn_permlane16_swap(a[0], bq[0], false, false);
        auto r1 = __builtin_amdgcn_permlane16_swap(a[1], bq[1], false, false);
        u32x4 t = {(unsigned)r0[0], (unsigned)r1[0], (unsigned)r0[1], (unsigned)r1[1]};
        st16_wt(vb_base + (size_t)bb * 8 * 28 * 1024 + c2 * 1024, t);
      };
#pragma unroll
      for (int n = 0; n < 3; ++n) vdma(0, n);
#pragma unroll
      for (int jr = 0; jr < 8; ++jr) wld(0, jr);
      wait_vm<8>();
      __syncthreads();
#pragma unroll
      for (int jr = 0; jr < 8; ++jr) wld(1, jr);
      f32x4 acc0[3][8], acc1[3][8];
#pragma unroll
      for (int mt = 0; mt < 3; ++mt)
#pragma unroll
        for (int c = 0; c < 8; ++c) { acc0[mt][c] = f32x4{0.f, 0.f, 0.f, 0.f}; acc1[mt][c] = f32x4{0.f, 0.f, 0.f, 0.f}; }
      u32x4 F[8][8];
      u32x4 va[2];
      va[0] = *(const u32x4*)vaddr(0, 0);
      wait_vm8<8>(R[0]);
      transpose8(R[0], F[0]);
      SB();
#pragma unroll
      for (int s = 0; s < 8; ++s) {
#pragma unroll
        for (int mt = 0; mt < 3; ++mt) {
          const int gidx = 3 * s + mt;
          const bool bar_after = (mt == 2) && (s == 3 || s == 7);
          if (mt < 2) va[(gidx + 1) & 1] = *(const u32x4*)vaddr(s, mt + 1);
          else if (!bar_after) va[(gidx + 1) & 1] = *(const u32x4*)vaddr(s + 1, 0);
          if (mt == 1 && s < 7) {
            if (s == 0) wait_vm8<3>(R[1]);
            if (s == 1) wait_vm8<6>(R[2]);
            if (s == 2) wait_vm8<6>(R[0]);
            if (s == 3) wait_vm8<6>(R[1]);
            if (s == 4) wait_vm8<3>(R[2]);
            if (s == 5) wait_vm8<3>(R[0]);
            if (s == 6) wait_vm8<0>(R[1]);
          }
          if (s < 6) {
            if (mt == 0) { wld(s + 2, 0); wld(s + 2, 1); wld(s + 2, 2); }
            if (mt == 1) { wld(s + 2, 3); wld(s + 2, 4); wld(s + 2, 5); }
            if (mt == 2) { wld(s + 2, 6); wld(s + 2, 7); }
          }
          if (mt == 2 && s < 3) { for (int n = 0; n < 3; ++n) vdma(s + 1, n); }
          if (mt >= 1 && s < 7) { transpose_q(R[(s + 1) % 3], F[s + 1], 2 * (mt - 1)); transpose_q(R[(s + 1) % 3], F[s + 1], 2 * (mt - 1) + 1); }
#pragma unroll
          for (int c = 0; c < 8; ++c) acc0[mt][c] = MFMA(va[gidx & 1], F[s][c], acc0[mt][c]);
          SB();
          if (bar_after) {
            __syncthreads();
            va[(gidx + 1) & 1] = *(const u32x4*)(s == 7 ? vaddr(0, 3) : vaddr(s + 1, 0));
          }
        }
      }
#pragma unroll
      for (int s = 0; s < 8; ++s) {
#pragma unroll
        for (int mt = 0; mt < 3; ++mt) {
          const int gidx = 3 * s + mt;
          if (mt < 2) va[(gidx + 1) & 1] = *(const u32x4*)vaddr(s, 3 + mt + 1);
          else if (s < 7) va[(gidx + 1) & 1] = *(const u32x4*)vaddr(s + 1, 3);
          if (gidx < 4) store_c2(acc0, 0, gidx);
#pragma unroll
          for (int c = 0; c < 8; ++c) acc1[mt][c] = MFMA(va[gidx & 1], F[s][c], acc1[mt][c]);
          if (s < 7) SB();
        }
      }
#pragma unroll
      for (int c2 = 0; c2 < 4; ++c2) store_c2(acc1, 1, c2);
    }
  }

  asm volatile("s_waitcnt vmcnt(0)" ::: "memory");
  __syncthreads();
  {
    const int prod = idx < 16 ? 2 * g + (idx & 1) : 16 + 2 * g + (idx & 1);
    if (wave == 0 && lane < 28) {
      unsigned long long* f = (unsigned long long*)(ws + WS_FL) + ((size_t)(p * 28 + lane) * 32 + prod);
      __hip_atomic_store(f, MAGIC, __ATOMIC_RELAXED, __HIP_MEMORY_SCOPE_AGENT);
    }
  }
  const int wgB = (idx - 16) * 8 + g, wgA = idx * 8 + g;
  const int task = idx >= 16 ? wgB : 128 + wgA;
  if (task < 224) {
    const int tb = (task & 7) + 8 * ((task >> 3) / 14);
    const int tt = (task >> 3) % 14;
    const int tm = tt >> 1, tcp = tt & 1;
    const int tp = tb >> 1;
    const int tloc = (tb & 1) * 14 + tt;
    unsigned long long* fl = (unsigned long long*)(ws + WS_FL) + (size_t)(tp * 28 + tloc) * 32;
    if (wave == 0) {
      for (unsigned spin = 0; spin < (1u << 22); ++spin) {
        unsigned long long v = MAGIC;
        if (lane < 32) v = __hip_atomic_load(fl + lane, __ATOMIC_RELAXED, __HIP_MEMORY_SCOPE_AGENT);
        if (__all(v == MAGIC)) break;
        __builtin_amdgcn_s_sleep(2);
      }
      if (lane < 32) __hip_atomic_store(fl + lane, 0ull, __ATOMIC_RELAXED, __HIP_MEMORY_SCOPE_AGENT);
    }
    __syncthreads();
    u32x4 pf[2][3][3], wf[2][3][2];
    const unsigned lo = lane * 16;
#pragma unroll
    for (int hp2 = 0; hp2 < 2; ++hp2) {
      const int hp = 2 * wave + hp2;
#pragma unroll
      for (int cc = 0; cc < 2; ++cc) {
        const int c2 = 2 * tcp + cc;
        gld16c(wf[hp2][0][cc], ws + WS_VA + (size_t)(((tb * HQ + 2 * hp) * 7 + tm) * 4 + c2) * 1024, lo);
        gld16c(wf[hp2][1][cc], ws + WS_VA + (size_t)(((tb * HQ + 2 * hp + 1) * 7 + tm) * 4 + c2) * 1024, lo);
        gld16c(wf[hp2][2][cc], ws + WS_VB + (size_t)(((tb * 8 + hp) * 7 + tm) * 4 + c2) * 1024, lo);
      }
#pragma unroll
      for (int qt = 0; qt < 3; ++qt) {
        gld16c(pf[hp2][0][qt], ws + WS_PA + (size_t)((tb * HQ + 2 * hp) * 3 + qt) * 1024, lo);
        gld16c(pf[hp2][1][qt], ws + WS_PA + (size_t)((tb * HQ + 2 * hp + 1) * 3 + qt) * 1024, lo);
        gld16c(pf[hp2][2][qt], ws + WS_PB + (size_t)((tb * 8 + hp) * 3 + qt) * 1024, lo);
      }
    }
    asm volatile("s_waitcnt vmcnt(0)"
                 : "+v"(pf[0][0][0]), "+v"(pf[0][0][1]), "+v"(pf[0][0][2]), "+v"(pf[0][1][0]), "+v"(pf[0][1][1]), "+v"(pf[0][1][2]),
                   "+v"(pf[0][2][0]), "+v"(pf[0][2][1]), "+v"(pf[0][2][2]), "+v"(pf[1][0][0]), "+v"(pf[1][0][1]), "+v"(pf[1][0][2]),
                   "+v"(pf[1][1][0]), "+v"(pf[1][1][1]), "+v"(pf[1][1][2]), "+v"(pf[1][2][0]), "+v"(pf[1][2][1]), "+v"(pf[1][2][2])
                 :: "memory");
    asm volatile(""
                 : "+v"(wf[0][0][0]), "+v"(wf[0][0][1]), "+v"(wf[0][1][0]), "+v"(wf[0][1][1]), "+v"(wf[0][2][0]), "+v"(wf[0][2][1]),
                   "+v"(wf[1][0][0]), "+v"(wf[1][0][1]), "+v"(wf[1][1][0]), "+v"(wf[1][1][1]), "+v"(wf[1][2][0]), "+v"(wf[1][2][1])
                 :: "memory");
#pragma unroll
    for (int qt = 0; qt < 3; ++qt)
#pragma unroll
      for (int cc = 0; cc < 2; ++cc) {
        f32x4 a = f32x4{0.f, 0.f, 0.f, 0.f};
#pragma unroll
        for (int hp2 = 0; hp2 < 2; ++hp2)
#pragma unroll
          for (int u = 0; u < 3; ++u) a = MFMA(pf[hp2][u][qt], wf[hp2][u][cc], a);
        *(f32x4*)(lds + ((wave * 3 + qt) * 2 + cc) * 1024 + lane * 16) = a;
      }
    __syncthreads();
    const int tid = threadIdx.x;
#pragma unroll
    for (int rep = 0; rep < 2; ++rep) {
      const int t = tid + 256 * rep;
      if (t < 384) {
        const int q = t >> 3, ip = t & 7;
        const int qt = q >> 4, g4 = (q >> 2) & 3, r = q & 3;
        float v[4] = {0.f, 0.f, 0.f, 0.f};
#pragma unroll
        for (int ww = 0; ww < 4; ++ww)
#pragma unroll
          for (int cc = 0; cc < 2; ++cc)
#pragma unroll
            for (int e = 0; e < 2; ++e)
              v[2 * cc + e] += *(const float*)(lds + ((ww * 3 + qt) * 2 + cc) * 1024 + ((g4 << 4) | (e << 3) | ip) * 16 + r * 4);
        u32x2 o = {pk2(v[0], v[1]), pk2(v[2], v[3])};
        *(u32x2*)(out + (size_t)(tb * SQ + q) * DM + 64 * tm + 8 * ip + 4 * tcp) = o;
      }
    }
  }
}

extern "C" void kernel_launch(void* const* d_in, const int* in_sizes, int n_in, void* d_out, int out_size, void* d_ws,
                              size_t ws_size, hipStream_t stream) {
  const uint16_t* x = (const uint16_t*)d_in[0];
  const uint16_t* kc = (const uint16_t*)d_in[1];
  const uint16_t* vc = (const uint16_t*)d_in[2];
  const uint16_t* wq = (const uint16_t*)d_in[3];
  const uint16_t* wo = (const uint16_t*)d_in[4];
  k1_attn<<<256, 256, 0, stream>>>(x, kc, vc, wq, wo, (char*)d_ws, (uint16_t*)d_out);
}
```

```cpp
#include <hip/hip_runtime.h>
#include <hip/hip_bf16.h>
#include <stdint.h>

typedef __attribute__((ext_vector_type(8))) __bf16 bf16x8;
typedef __attribute__((ext_vector_type(2))) __bf16 bf16x2;
typedef __attribute__((ext_vector_type(4))) float f32x4;
typedef __attribute__((ext_vector_type(2))) float f32x2;
typedef __attribute__((ext_vector_type(4))) unsigned u32x4;
typedef __attribute__((ext_vector_type(2))) unsigned u32x2;

#define DI __device__ __forceinline__
#define MFMA(a, b, c) __builtin_amdgcn_mfma_f32_16x16x32_bf16(__builtin_bit_cast(bf16x8, (a)), __builtin_bit_cast(bf16x8, (b)), (c), 0, 0, 0)
#define SB() __builtin_amdgcn_sched_barrier(0)
#define NTS ""

constexpr int NB = 16, SQ = 48, DM = 448, HQ = 16, HKV = 8, SK = 48, DH = 256;

constexpr size_t WS_PA = 0;
constexpr size_t WS_PB = WS_PA + (size_t)NB * HQ * 3 * 1024;
constexpr size_t WS_VA = WS_PB + (size_t)NB * 8 * 3 * 1024;
constexpr size_t WS_VB = WS_VA + (size_t)NB * HQ * 28 * 1024;
constexpr size_t WS_FL = WS_VB + (size_t)NB * 8 * 28 * 1024;
constexpr unsigned long long MAGIC = 0x9E3779B97F4A7C15ull;

DI unsigned pk2(float a, float b) {
  f32x2 v = {a, b};
  bf16x2 r = __builtin_convertvector(v, bf16x2);
  return __builtin_bit_cast(unsigned, r);
}
DI u32x2 pk4(f32x4 v) {
  u32x2 r = {pk2(v[0], v[1]), pk2(v[2], v[3])};
  return r;
}
DI u32x4 cat(u32x2 a, u32x2 b) {
  u32x4 r = {a[0], a[1], b[0], b[1]};
  return r;
}
DI int swap02(int i) { return (i & 10) | ((i & 1) << 2) | ((i >> 2) & 1); }
DI void st16_wt(char* p, u32x4 v) { asm volatile("global_store_dwordx4 %0, %1, off sc1\n\ts_nop 1" ::"v"(p), "v"(v) : "memory"); }
DI void st8_wt(char* p, u32x2 v) { asm volatile("global_store_dwordx2 %0, %1, off sc1\n\ts_nop 1" ::"v"(p), "v"(v) : "memory"); }

DI float red_max(float m) {
  unsigned u = __builtin_bit_cast(unsigned, m);
  auto r = __builtin_amdgcn_permlane32_swap(u, u, false, false);
  m = fmaxf(__builtin_bit_cast(float, (unsigned)r[0]), __builtin_bit_cast(float, (unsigned)r[1]));
  u = __builtin_bit_cast(unsigned, m);
  auto r2 = __builtin_amdgcn_permlane16_swap(u, u, false, false);
  return fmaxf(__builtin_bit_cast(float, (unsigned)r2[0]), __builtin_bit_cast(float, (unsigned)r2[1]));
}
DI float red_sum(float m) {
  unsigned u = __builtin_bit_cast(unsigned, m);
  auto r = __builtin_amdgcn_permlane32_swap(u, u, false, false);
  m = __builtin_bit_cast(float, (unsigned)r[0]) + __builtin_bit_cast(float, (unsigned)r[1]);
  u = __builtin_bit_cast(unsigned, m);
  auto r2 = __builtin_amdgcn_permlane16_swap(u, u, false, false);
  return __builtin_bit_cast(float, (unsigned)r2[0]) + __builtin_bit_cast(float, (unsigned)r2[1]);
}

DI void transpose8(const u32x4 (&R)[8], u32x4 (&F)[8]) {
#pragma unroll
  for (int t = 0; t < 4; ++t)
#pragma unroll
    for (int jj = 0; jj < 4; ++jj) {
      F[2 * t][jj] = __builtin_amdgcn_perm(R[2 * jj + 1][t], R[2 * jj][t], 0x05040100u);
      F[2 * t + 1][jj] = __builtin_amdgcn_perm(R[2 * jj + 1][t], R[2 * jj][t], 0x07060302u);
    }
}

constexpr bool ROT = true;
constexpr int CODE_LINES = 216;
constexpr int LDS_X = 0;
constexpr int LDS_K = 96 * 896;
constexpr int LDS_V = 0;
constexpr int LDS_TOTAL = LDS_K + 96 * 512;

DI void gld16(u32x4& dst, const char* sbase, unsigned voff) {
  asm volatile("global_load_dwordx4 %0, %1, %2" NTS : "=v"(dst) : "v"(voff), "s"(sbase) : "memory");
}
DI void gld16c(u32x4& dst, const char* sbase, unsigned voff) {
  asm volatile("global_load_dwordx4 %0, %1, %2 sc1" : "=v"(dst) : "v"(voff), "s"(sbase) : "memory");
}
DI void dma16(unsigned lds_dst, const char* sbase, unsigned voff) {
  unsigned keep;
  asm volatile("s_mov_b32 %0, m0\n\ts_mov_b32 m0, %3\n\ts_nop 0\n\tglobal_load_lds_dwordx4 %1, %2\n\ts_mov_b32 m0, %0"
               : "=&s"(keep) : "v"(voff), "s"(sbase), "s"(lds_dst) : "memory");
}
DI void dma16nt(unsigned lds_dst, const char* sbase, unsigned voff) {
  unsigned keep;
  asm volatile("s_mov_b32 %0, m0\n\ts_mov_b32 m0, %3\n\ts_nop 0\n\tglobal_load_lds_dwordx4 %1, %2" NTS "\n\ts_mov_b32 m0, %0"
               : "=&s"(keep) : "v"(voff), "s"(sbase), "s"(lds_dst) : "memory");
}
template <int N>
DI void wait_vm() { asm volatile("s_waitcnt vmcnt(%0)" ::"i"(N) : "memory"); }
template <int N>
DI void wait_vm8(u32x4 (&R)[8]) {
  asm volatile("s_waitcnt vmcnt(%8)"
               : "+v"(R[0]), "+v"(R[1]), "+v"(R[2]), "+v"(R[3]), "+v"(R[4]), "+v"(R[5]), "+v"(R[6]), "+v"(R[7])
               : "i"(N) : "memory");
}

template <int OFF>
DI void gld16o(u32x4& dst, const char* sbase, unsigned voff) {
  asm volatile("global_load_dwordx4 %0, %1, %2 offset:%3" NTS : "=v"(dst) : "v"(voff), "s"(sbase), "i"(OFF) : "memory");
}
DI void transpose_q(const u32x4 (&R)[8], u32x4 (&F)[8], int t) {
#pragma unroll
  for (int jj = 0; jj < 4; ++jj) {
    F[2 * t][jj] = __builtin_amdgcn_perm(R[2 * jj + 1][t], R[2 * jj][t], 0x05040100u);
    F[2 * t + 1][jj] = __builtin_amdgcn_perm(R[2 * jj + 1][t], R[2 * jj][t], 0x07060302u);
  }
}

DI void touch(unsigned& dst, const char* p) { asm volatile("global_load_dword %0, %1, off" : "+v"(dst) : "v"(p) : "memory"); }
__global__ __launch_bounds__(256, 1) void k1_attn(const uint16_t* __restrict__ x, const uint16_t* __restrict__ kc,
                                                  const uint16_t* __restrict__ vc, const uint16_t* __restrict__ wq,
                                                  const uint16_t* __restrict__ wo, char* __restrict__ ws,
                                                  uint16_t* __restrict__ out) {
  __shared__ __attribute__((aligned(16))) char lds[LDS_TOTAL];
  unsigned long long pc0;
  asm volatile("s_getpc_b64 %0" : "=s"(pc0));
  const int bid = blockIdx.x;
  const int g = bid & 7;
  const int idx = bid >> 3;
  const int p = (idx & 15) >> 1;
  const int wave = __builtin_amdgcn_readfirstlane(threadIdx.x >> 6);
  const int lane = threadIdx.x & 63;
  const int i = lane & 15, gq = lane >> 4;
  const int l8 = lane >> 3, e8 = lane & 7;
  const unsigned lds0 = (unsigned)(size_t)lds;
  if (idx < 16) {
    const int h = 2 * g + (idx & 1);
    const int half = wave & 1, kh = wave >> 1;
    auto gline = [&](int slot) {
      int r = slot + (ROT ? p : 0);
      r = r >= 7 ? r - 7 : r;
      r = r >= 7 ? r - 7 : r;
      return r;
    };
    auto line_of = [&](int t) { return 3 * kh + t; };
    auto ks_of = [&](int s) { return s < 6 ? 2 * gline(line_of(s >> 1)) + (s & 1) : 2 * gline(6) + kh; };
    const char* xs = (const char*)(x + (size_t)(2 * p) * SQ * DM);
    const unsigned xoff0 = (unsigned)(l8 * 896 + ((e8 ^ (l8 >> 1)) * 16));
    const unsigned xoff1 = (unsigned)(l8 * 896 + ((e8 ^ ((l8 >> 1) | 4)) * 16));
    auto xdma = [&](int L, int rg) { dma16(lds0 + LDS_X + L * 12288 + rg * 1024, xs + (size_t)rg * 8 * 896 + gline(L) * 128, (rg & 1) ? xoff1 : xoff0); };
    const char* kbase = (const char*)(kc + (size_t)((2 * p + (wave >> 1)) * HKV + g) * SK * DH) + (wave & 1) * 12288;
    auto kdma = [&](int n) {
      const int row = (wave * 12 + n) * 2 + (lane >> 5);
      dma16nt(lds0 + LDS_K + (wave * 12 + n) * 1024, kbase + n * 1024, (unsigned)((lane >> 5) * 512 + (((lane & 31) ^ swap02(row & 15)) * 16)));
    };
    unsigned woff[8];
#pragma unroll
    for (int j = 0; j < 8; ++j) woff[j] = (unsigned)((8 * gq + j) * (HQ * DH) + 8 * i) * 2u;
    const char* wbase = (const char*)(wq + h * DH + 128 * half);
    u32x4 R[3][8];
    auto wld = [&](int s, int j) { gld16(R[s % 3][j], wbase + (size_t)(32 * ks_of(s)) * (HQ * DH * 2), woff[j]); };
    auto xaddr = [&](int s, int qt) {
      const int L = s < 6 ? line_of(s >> 1) : 6;
      const int eo = s < 6 ? 4 * (s & 1) : 4 * kh;
      const int row = 16 * qt + i;
      return lds + LDS_X + L * 12288 + row * 128 + (((eo + gq) ^ ((row >> 1) & 7)) * 16);
    };
#pragma unroll
    for (int n = 0; n < 6; ++n) xdma(line_of(0), 6 * half + n);
#pragma unroll
    for (int j = 0; j < 8; ++j) wld(0, j);
    wait_vm<8>();
    __syncthreads();
#pragma unroll
    for (int j = 0; j < 8; ++j) wld(1, j);

    f32x4 acc[8][6];
#pragma unroll
    for (int c = 0; c < 8; ++c)
#pragma unroll
      for (int qt = 0; qt < 6; ++qt) acc[c][qt] = f32x4{0.f, 0.f, 0.f, 0.f};
    u32x4 F[2][8];
    u32x4 xb[2];
    xb[0] = *(const u32x4*)xaddr(0, 0);
    wait_vm8<8>(R[0]);
    transpose8(R[0], F[0]);
    SB();
#pragma unroll
    for (int s = 0; s < 7; ++s) {
#pragma unroll
      for (int qt = 0; qt < 6; ++qt) {
        const bool bar_after = (qt == 5) && (s == 1 || s == 3 || s == 5);
        if (qt < 5) xb[(qt + 1) & 1] = *(const u32x4*)xaddr(s, qt + 1);
        else if (s < 6 && !bar_after) xb[0] = *(const u32x4*)xaddr(s + 1, 0);
        if (qt == 2 && s < 6) {
          if (s == 0) wait_vm8<4>(R[1]);
          if (s == 1) wait_vm8<10>(R[2]);
          if (s == 2) wait_vm8<10>(R[0]);
          if (s == 3) wait_vm8<10>(R[1]);
          if (s == 4) wait_vm8<13>(R[2]);
          if (s == 5) wait_vm8<0>(R[0]);
        }
        if (qt == 0 && s == 4) { kdma(9); kdma(10); kdma(11); }
        if (qt < 4 && s < 5) { wld(s + 2, 2 * qt); wld(s + 2, 2 * qt + 1); }
        if (qt >= 4 && s == 0) { for (int n = 0; n < 3; ++n) xdma(line_of(1), 6 * half + 3 * (qt - 4) + n); }
        if (qt >= 4 && s == 1) { for (int n = 0; n < 3; ++n) xdma(line_of(2), 6 * half + 3 * (qt - 4) + n); }
        if (qt == 4 && s == 2) { for (int n = 0; n < 3; ++n) xdma(6, 3 * wave + n); }
        if (qt == 5 && s == 2) { kdma(0); kdma(1); kdma(2); }
        if (qt >= 4 && s == 3) { kdma(3 + 3 * (qt - 4)); kdma(4 + 3 * (qt - 4)); kdma(5 + 3 * (qt - 4)); }
        if (qt >= 2 && s < 6) transpose_q(R[(s + 1) % 3], F[(s + 1) & 1], qt - 2);
#pragma unroll
        for (int c = 0; c < 8; ++c) acc[c][qt] = MFMA(F[s & 1][c], xb[qt & 1], acc[c][qt]);
        if (!(s == 6)) SB();
        if (bar_after) {
          if (s == 1) wait_vm<14>();
          __syncthreads();
          xb[0] = *(const u32x4*)xaddr(s + 1, 0);
        }
      }
    }
#pragma unroll
    for (int bb = 0; bb < 2; ++bb) {
      u32x4 kf[3][4];
#pragma unroll
      for (int kt = 0; kt < 3; ++kt)
#pragma unroll
        for (int s = 0; s < 4; ++s) {
          const int row = 48 * bb + 16 * kt + i;
          kf[kt][s] = *(const u32x4*)(lds + LDS_K + row * 512 + (((16 * half + 4 * gq + s) ^ swap02(i)) * 16));
        }
#pragma unroll
      for (int qt = 0; qt < 3; ++qt) {
        const int q6 = 3 * bb + qt;
        f32x4 sacc[3];
#pragma unroll
        for (int kt = 0; kt < 3; ++kt) sacc[kt] = f32x4{0.f, 0.f, 0.f, 0.f};
#pragma unroll
        for (int s = 0; s < 4; ++s) {
          u32x4 qf = {pk2(acc[0][q6][s], acc[1][q6][s]), pk2(acc[2][q6][s], acc[3][q6][s]),
                      pk2(acc[4][q6][s], acc[5][q6][s]), pk2(acc[6][q6][s], acc[7][q6][s])};
#pragma unroll
          for (int kt = 0; kt < 3; ++kt) sacc[kt] = MFMA(kf[kt][s], qf, sacc[kt]);
        }
#pragma unroll
        for (int kt = 0; kt < 3; ++kt)
          *(f32x4*)(lds + LDS_X + (wave * 18 + (bb * 3 + qt) * 3 + kt) * 1024 + lane * 16) = sacc[kt];
      }
    }
    __syncthreads();
    {
      f32x4 s[2][3];
      const int u1 = wave < 2 ? wave + 4 : wave;
#pragma unroll
      for (int rep = 0; rep < 2; ++rep) {
        const int u = rep ? u1 : wave;
#pragma unroll
        for (int kt = 0; kt < 3; ++kt) {
          s[rep][kt] = *(const f32x4*)(lds + LDS_X + (0 * 18 + u * 3 + kt) * 1024 + lane * 16);
#pragma unroll
          for (int ww = 1; ww < 4; ++ww) s[rep][kt] += *(const f32x4*)(lds + LDS_X + (ww * 18 + u * 3 + kt) * 1024 + lane * 16);
        }
      }
      const float sc = 0.0625f * 1.44269504088896340736f;
#pragma unroll
      for (int rep = 0; rep < 2; ++rep) {
        float m = -INFINITY;
#pragma unroll
        for (int kt = 0; kt < 3; ++kt)
#pragma unroll
          for (int r = 0; r < 4; ++r) m = fmaxf(m, s[rep][kt][r]);
        m = red_max(m) * sc;
        float sum = 0.f;
#pragma unroll
        for (int kt = 0; kt < 3; ++kt)
#pragma unroll
          for (int r = 0; r < 4; ++r) {
            s[rep][kt][r] = __builtin_amdgcn_exp2f(__builtin_fmaf(s[rep][kt][r], sc, -m));
            sum += s[rep][kt][r];
          }
        sum = red_sum(sum);
        const float inv = 1.0f / sum;
#pragma unroll
        for (int kt = 0; kt < 3; ++kt) s[rep][kt] *= inv;
      }
#pragma unroll
      for (int rep = 0; rep < 2; ++rep) {
        if (rep == 0 || wave < 2) {
          const int u = rep ? u1 : wave;
          const int bb = u >= 3 ? 1 : 0, qt = u - 3 * bb;
          const int b = 2 * p + bb;
          st16_wt(ws + WS_PA + (size_t)((b * HQ + h) * 3 + qt) * 1024 + lane * 16, cat(pk4(s[rep][0]), pk4(s[rep][1])));
          st8_wt(ws + WS_PB + (size_t)((b * 8 + g) * 3 + qt) * 1024 + (((2 * (h & 1) + (gq >> 1)) << 4) | i) * 16 + (gq & 1) * 8, pk4(s[rep][2]));
        }
      }
    }
  } else {
    const int j = 4 * (idx & 1) + wave;
    const bool active = j < 7;
    auto line_of = [&](int t) { return ROT ? (t + p) & 3 : t; };
    const unsigned voff0 = (unsigned)(l8 * 512 + ((e8 ^ (l8 >> 1)) * 16));
    const unsigned voff1 = (unsigned)(l8 * 512 + ((e8 ^ ((l8 >> 1) | 4)) * 16));
    auto vdma = [&](int d, int n) {
      const int q = 3 * wave + n;
      const int t = 2 * (d & 1) + (q >= 6 ? 1 : 0);
      const int rgl = q >= 6 ? q - 6 : q;
      const int bb = d >> 1;
      const int L = line_of(t);
      const char* vs_ = (const char*)(vc + (size_t)((2 * p + bb) * HKV + g) * SK * DH);
      dma16nt(lds0 + LDS_V + L * 12288 + (6 * bb + rgl) * 1024, vs_ + (size_t)rgl * 8 * 512 + L * 128, (rgl & 1) ? voff1 : voff0);
    };
    if (!active) {
      unsigned sink = 0;
#pragma unroll
      for (int n = 0; n < 4; ++n) {
        const int ln = n * 64 + lane;
        touch(sink, (const char*)pc0 + (size_t)(ln < CODE_LINES ? ln : 0) * 128);
      }
#pragma unroll
      for (int d = 0; d < 4; ++d)
#pragma unroll
        for (int n = 0; n < 3; ++n) vdma(d, n);
      wait_vm<9>();
      asm volatile("" : "+v"(sink));
      __syncthreads();
      wait_vm<6>();
      __syncthreads();
      wait_vm<0>();
      __syncthreads();
    } else {
      const int np = 128 * j + 8 * i;
      const int hl = np >= DM ? 1 : 0;
      const int n0 = np - DM * hl;
      const unsigned woffa = (unsigned)((hl * DH + 8 * gq) * DM + n0) * 2u;
      const unsigned woffb = woffa + 4 * DM * 2;
      const char* wbase = (const char*)(wo + (size_t)(2 * g) * DH * DM);
      u32x4 R[3][8];
      auto wld = [&](int s, int jr) {
        const char* sb = wbase + (size_t)(32 * (2 * line_of(s >> 1) + (s & 1))) * (DM * 2);
        if (jr == 0) gld16o<0>(R[s % 3][0], sb, woffa);
        if (jr == 1) gld16o<896>(R[s % 3][1], sb, woffa);
        if (jr == 2) gld16o<1792>(R[s % 3][2], sb, woffa);
        if (jr == 3) gld16o<2688>(R[s % 3][3], sb, woffa);
        if (jr == 4) gld16o<0>(R[s % 3][4], sb, woffb);
        if (jr == 5) gld16o<896>(R[s % 3][5], sb, woffb);
        if (jr == 6) gld16o<1792>(R[s % 3][6], sb, woffb);
        if (jr == 7) gld16o<2688>(R[s % 3][7], sb, woffb);
      };
      auto vaddr = [&](int s, int mt) {
        const int row = 16 * mt + i;
        return lds + LDS_V + line_of(s >> 1) * 12288 + row * 128 + (((4 * (s & 1) + gq) ^ ((row >> 1) & 7)) * 16);
      };
      const int m = n0 >> 6, ip = (n0 >> 3) & 7;
      const int h = 2 * g + hl;
      char* va_base = ws + WS_VA + (size_t)(((2 * p * HQ + h) * 7 + m) * 4) * 1024 + ((gq << 4) | ip) * 16;
      char* vb_base = ws + WS_VB + (size_t)(((2 * p * 8 + g) * 7 + m) * 4) * 1024 + (((2 * hl + (gq >> 1)) << 4) | ((gq & 1) << 3) | ip) * 16;
      auto store_c2 = [&](f32x4 (&acc)[3][8], int bb, int c2) {
#pragma unroll
        for (int e = 0; e < 2; ++e)
          st16_wt(va_base + (size_t)bb * HQ * 28 * 1024 + c2 * 1024 + e * 128, cat(pk4(acc[0][2 * c2 + e]), pk4(acc[1][2 * c2 + e])));
        u32x2 a = pk4(acc[2][2 * c2]), bq = pk4(acc[2][2 * c2 + 1]);
        auto r0 = __builtin_amdgcn_permlane16_swap(a[0], bq[0], false, false);
        auto r1 = __builtin_amdgcn_permlane16_swap(a[1], bq[1], false, false);
        u32x4 t = {(unsigned)r0[0], (unsigned)r1[0], (unsigned)r0[1], (unsigned)r1[1]};
        st16_wt(vb_base + (size_t)bb * 8 * 28 * 1024 + c2 * 1024, t);
      };
#pragma unroll
      for (int n = 0; n < 3; ++n) vdma(0, n);
#pragma unroll
      for (int jr = 0; jr < 8; ++jr) wld(0, jr);
      wait_vm<8>();
      __syncthreads();
#pragma unroll
      for (int jr = 0; jr < 8; ++jr) wld(1, jr);
      f32x4 acc0[3][8], acc1[3][8];
#pragma unroll
      for (int mt = 0; mt < 3; ++mt)
#pragma unroll
        for (int c = 0; c < 8; ++c) { acc0[mt][c] = f32x4{0.f, 0.f, 0.f, 0.f}; acc1[mt][c] = f32x4{0.f, 0.f, 0.f, 0.f}; }
      u32x4 F[8][8];
      u32x4 va[2];
      va[0] = *(const u32x4*)vaddr(0, 0);
      wait_vm8<8>(R[0]);
      transpose8(R[0], F[0]);
      SB();
#pragma unroll
      for (int s = 0; s < 8; ++s) {
#pragma unroll
        for (int mt = 0; mt < 3; ++mt) {
          const int gidx = 3 * s + mt;
          const bool bar_after = (mt == 2) && (s == 3 || s == 7);
          if (mt < 2) va[(gidx + 1) & 1] = *(const u32x4*)vaddr(s, mt + 1);
          else if (!bar_after) va[(gidx + 1) & 1] = *(const u32x4*)vaddr(s + 1, 0);
          if (mt == 1 && s < 7) {
            if (s == 0) wait_vm8<3>(R[1]);
            if (s == 1) wait_vm8<6>(R[2]);
            if (s == 2) wait_vm8<6>(R[0]);
            if (s == 3) wait_vm8<6>(R[1]);
            if (s == 4) wait_vm8<3>(R[2]);
            if (s == 5) wait_vm8<3>(R[0]);
            if (s == 6) wait_vm8<0>(R[1]);
          }
          if (s < 6) {
            if (mt == 0) { wld(s + 2, 0); wld(s + 2, 1); wld(s + 2, 2); }
            if (mt == 1) { wld(s + 2, 3); wld(s + 2, 4); wld(s + 2, 5); }
            if (mt == 2) { wld(s + 2, 6); wld(s + 2, 7); }
          }
          if (mt == 2 && s < 3) { for (int n = 0; n < 3; ++n) vdma(s + 1, n); }
          if (mt >= 1 && s < 7) { transpose_q(R[(s + 1) % 3], F[s + 1], 2 * (mt - 1)); transpose_q(R[(s + 1) % 3], F[s + 1], 2 * (mt - 1) + 1); }
#pragma unroll
          for (int c = 0; c < 8; ++c) acc0[mt][c] = MFMA(va[gidx & 1], F[s][c], acc0[mt][c]);
          SB();
          if (bar_after) {
            __syncthreads();
            va[(gidx + 1) & 1] = *(const u32x4*)(s == 7 ? vaddr(0, 3) : vaddr(s + 1, 0));
          }
        }
      }
#pragma unroll
      for (int s = 0; s < 8; ++s) {
#pragma unroll
        for (int mt = 0; mt < 3; ++mt) {
          const int gidx = 3 * s + mt;
          if (mt < 2) va[(gidx + 1) & 1] = *(const u32x4*)vaddr(s, 3 + mt + 1);
          else if (s < 7) va[(gidx + 1) & 1] = *(const u32x4*)vaddr(s + 1, 3);
          if (gidx < 4) store_c2(acc0, 0, gidx);
#pragma unroll
          for (int c = 0; c < 8; ++c) acc1[mt][c] = MFMA(va[gidx & 1], F[s][c], acc1[mt][c]);
          if (s < 7) SB();
        }
      }
#pragma unroll
      for (int c2 = 0; c2 < 4; ++c2) store_c2(acc1, 1, c2);
    }
  }

  asm volatile("s_waitcnt vmcnt(0)" ::: "memory");
  __syncthreads();
  {
    const int prod = idx < 16 ? 2 * g + (idx & 1) : 16 + 2 * g + (idx & 1);
    if (wave == 0 && lane < 28) {
      unsigned long long* f = (unsigned long long*)(ws + WS_FL) + ((size_t)(p * 28 + lane) * 32 + prod);
      __hip_atomic_store(f, MAGIC, __ATOMIC_RELAXED, __HIP_MEMORY_SCOPE_AGENT);
    }
  }
  const int wgB = (idx - 16) * 8 + g, wgA = idx * 8 + g;
  const int task = idx >= 16 ? wgB : 128 + wgA;
  if (task < 224) {
    const int tb = (task & 7) + 8 * ((task >> 3) / 14);
    const int tt = (task >> 3) % 14;
    const int tm = tt >> 1, tcp = tt & 1;
    const int tp = tb >> 1;
    const int tloc = (tb & 1) * 14 + tt;
    unsigned long long* fl = (unsigned long long*)(ws + WS_FL) + (size_t)(tp * 28 + tloc) * 32;
    if (wave == 0) {
      for (unsigned spin = 0; spin < (1u << 22); ++spin) {
        unsigned long long v = MAGIC;
        if (lane < 32) v = __hip_atomic_load(fl + lane, __ATOMIC_RELAXED, __HIP_MEMORY_SCOPE_AGENT);
        if (__all(v == MAGIC)) break;
        __builtin_amdgcn_s_sleep(2);
      }
      if (lane < 32) __hip_atomic_store(fl + lane, 0ull, __ATOMIC_RELAXED, __HIP_MEMORY_SCOPE_AGENT);
    }
    __syncthreads();
    u32x4 pf[2][3][3], wf[2][3][2];
    const unsigned lo = lane * 16;
#pragma unroll
    for (int hp2 = 0; hp2 < 2; ++hp2) {
      const int hp = 2 * wave + hp2;
#pragma unroll
      for (int cc = 0; cc < 2; ++cc) {
        const int c2 = 2 * tcp + cc;
        gld16c(wf[hp2][0][cc], ws + WS_VA + (size_t)(((tb * HQ + 2 * hp) * 7 + tm) * 4 + c2) * 1024, lo);
        gld16c(wf[hp2][1][cc], ws + WS_VA + (size_t)(((tb * HQ + 2 * hp + 1) * 7 + tm) * 4 + c2) * 1024, lo);
        gld16c(wf[hp2][2][cc], ws + WS_VB + (size_t)(((tb * 8 + hp) * 7 + tm) * 4 + c2) * 1024, lo);
      }
#pragma unroll
      for (int qt = 0; qt < 3; ++qt) {
        gld16c(pf[hp2][0][qt], ws + WS_PA + (size_t)((tb * HQ + 2 * hp) * 3 + qt) * 1024, lo);
        gld16c(pf[hp2][1][qt], ws + WS_PA + (size_t)((tb * HQ + 2 * hp + 1) * 3 + qt) * 1024, lo);
        gld16c(pf[hp2][2][qt], ws + WS_PB + (size_t)((tb * 8 + hp) * 3 + qt) * 1024, lo);
      }
    }
    asm volatile("s_waitcnt vmcnt(0)"
                 : "+v"(pf[0][0][0]), "+v"(pf[0][0][1]), "+v"(pf[0][0][2]), "+v"(pf[0][1][0]), "+v"(pf[0][1][1]), "+v"(pf[0][1][2]),
                   "+v"(pf[0][2][0]), "+v"(pf[0][2][1]), "+v"(pf[0][2][2]), "+v"(pf[1][0][0]), "+v"(pf[1][0][1]), "+v"(pf[1][0][2]),
                   "+v"(pf[1][1][0]), "+v"(pf[1][1][1]), "+v"(pf[1][1][2]), "+v"(pf[1][2][0]), "+v"(pf[1][2][1]), "+v"(pf[1][2][2])
                 :: "memory");
    asm volatile(""
                 : "+v"(wf[0][0][0]), "+v"(wf[0][0][1]), "+v"(wf[0][1][0]), "+v"(wf[0][1][1]), "+v"(wf[0][2][0]), "+v"(wf[0][2][1]),
                   "+v"(wf[1][0][0]), "+v"(wf[1][0][1]), "+v"(wf[1][1][0]), "+v"(wf[1][1][1]), "+v"(wf[1][2][0]), "+v"(wf[1][2][1])
                 :: "memory");
#pragma unroll
    for (int qt = 0; qt < 3; ++qt)
#pragma unroll
      for (int cc = 0; cc < 2; ++cc) {
        f32x4 a = f32x4{0.f, 0.f, 0.f, 0.f};
#pragma unroll
        for (int hp2 = 0; hp2 < 2; ++hp2)
#pragma unroll
          for (int u = 0; u < 3; ++u) a = MFMA(pf[hp2][u][qt], wf[hp2][u][cc], a);
        *(f32x4*)(lds + ((wave * 3 + qt) * 2 + cc) * 1024 + lane * 16) = a;
      }
    __syncthreads();
    const int tid = threadIdx.x;
#pragma unroll
    for (int rep = 0; rep < 2; ++rep) {
      const int t = tid + 256 * rep;
      if (t < 384) {
        const int q = t >> 3, ip = t & 7;
        const int qt = q >> 4, g4 = (q >> 2) & 3, r = q & 3;
        float v[4] = {0.f, 0.f, 0.f, 0.f};
#pragma unroll
        for (int ww = 0; ww < 4; ++ww)
#pragma unroll
          for (int cc = 0; cc < 2; ++cc)
#pragma unroll
            for (int e = 0; e < 2; ++e)
              v[2 * cc + e] += *(const float*)(lds + ((ww * 3 + qt) * 2 + cc) * 1024 + ((g4 << 4) | (e << 3) | ip) * 16 + r * 4);
        u32x2 o = {pk2(v[0], v[1]), pk2(v[2], v[3])};
        *(u32x2*)(out + (size_t)(tb * SQ + q) * DM + 64 * tm + 8 * ip + 4 * tcp) = o;
      }
    }
  }
}

extern "C" void kernel_launch(void* const* d_in, const int* in_sizes, int n_in, void* d_out, int out_size, void* d_ws,
                              size_t ws_size, hipStream_t stream) {
  const uint16_t* x = (const uint16_t*)d_in[0];
  const uint16_t* kc = (const uint16_t*)d_in[1];
  const uint16_t* vc = (const uint16_t*)d_in[2];
  const uint16_t* wq = (const uint16_t*)d_in[3];
  const uint16_t* wo = (const uint16_t*)d_in[4];
  k1_attn<<<256, 256, 0, stream>>>(x, kc, vc, wq, wo, (char*)d_ws, (uint16_t*)d_out);
}
```

```cpp
#include <hip/hip_runtime.h>
#include <hip/hip_bf16.h>
#include <stdint.h>

typedef __attribute__((ext_vector_type(8))) __bf16 bf16x8;
typedef __attribute__((ext_vector_type(2))) __bf16 bf16x2;
typedef __attribute__((ext_vector_type(4))) float f32x4;
typedef __attribute__((ext_vector_type(2))) float f32x2;
typedef __attribute__((ext_vector_type(4))) unsigned u32x4;
typedef __attribute__((ext_vector_type(2))) unsigned u32x2;

#define DI __device__ __forceinline__
#define MFMA(a, b, c) __builtin_amdgcn_mfma_f32_16x16x32_bf16(__builtin_bit_cast(bf16x8, (a)), __builtin_bit_cast(bf16x8, (b)), (c), 0, 0, 0)
#define SB() __builtin_amdgcn_sched_barrier(0)
#define NTS ""

constexpr int NB = 16, SQ = 48, DM = 448, HQ = 16, HKV = 8, SK = 48, DH = 256;

constexpr size_t WS_PA = 0;
constexpr size_t WS_PB = WS_PA + (size_t)NB * HQ * 3 * 1024;
constexpr size_t WS_VA = WS_PB + (size_t)NB * 8 * 3 * 1024;
constexpr size_t WS_VB = WS_VA + (size_t)NB * HQ * 28 * 1024;
constexpr size_t WS_FL = WS_VB + (size_t)NB * 8 * 28 * 1024;
constexpr unsigned long long MAGIC = 0x9E3779B97F4A7C15ull;

DI unsigned pk2(float a, float b) {
  f32x2 v = {a, b};
  bf16x2 r = __builtin_convertvector(v, bf16x2);
  return __builtin_bit_cast(unsigned, r);
}
DI u32x2 pk4(f32x4 v) {
  u32x2 r = {pk2(v[0], v[1]), pk2(v[2], v[3])};
  return r;
}
DI u32x4 cat(u32x2 a, u32x2 b) {
  u32x4 r = {a[0], a[1], b[0], b[1]};
  return r;
}
DI int swap02(int i) { return (i & 10) | ((i & 1) << 2) | ((i >> 2) & 1); }
DI void st16_wt(char* p, u32x4 v) { asm volatile("global_store_dwordx4 %0, %1, off sc1\n\ts_nop 1" ::"v"(p), "v"(v) : "memory"); }
DI void st8_wt(char* p, u32x2 v) { asm volatile("global_store_dwordx2 %0, %1, off sc1\n\ts_nop 1" ::"v"(p), "v"(v) : "memory"); }

DI float red_max(float m) {
  unsigned u = __builtin_bit_cast(unsigned, m);
  auto r = __builtin_amdgcn_permlane32_swap(u, u, false, false);
  m = fmaxf(__builtin_bit_cast(float, (unsigned)r[0]), __builtin_bit_cast(float, (unsigned)r[1]));
  u = __builtin_bit_cast(unsigned, m);
  auto r2 = __builtin_amdgcn_permlane16_swap(u, u, false, false);
  return fmaxf(__builtin_bit_cast(float, (unsigned)r2[0]), __builtin_bit_cast(float, (unsigned)r2[1]));
}
DI float red_sum(float m) {
  unsigned u = __builtin_bit_cast(unsigned, m);
  auto r = __builtin_amdgcn_permlane32_swap(u, u, false, false);
  m = __builtin_bit_cast(float, (unsigned)r[0]) + __builtin_bit_cast(float, (unsigned)r[1]);
  u = __builtin_bit_cast(unsigned, m);
  auto r2 = __builtin_amdgcn_permlane16_swap(u, u, false, false);
  return __builtin_bit_cast(float, (unsigned)r2[0]) + __builtin_bit_cast(float, (unsigned)r2[1]);
}

DI void transpose8(const u32x4 (&R)[8], u32x4 (&F)[8]) {
#pragma unroll
  for (int t = 0; t < 4; ++t)
#pragma unroll
    for (int jj = 0; jj < 4; ++jj) {
      F[2 * t][jj] = __builtin_amdgcn_perm(R[2 * jj + 1][t], R[2 * jj][t], 0x05040100u);
      F[2 * t + 1][jj] = __builtin_amdgcn_perm(R[2 * jj + 1][t], R[2 * jj][t], 0x07060302u);
    }
}

constexpr bool ROT = true;
constexpr int CODE_LINES = 216;
constexpr int LDS_X = 0;
constexpr int LDS_K = 96 * 896;
constexpr int LDS_V = 0;
constexpr int LDS_TOTAL = LDS_K + 96 * 512;

DI void gld16(u32x4& dst, const char* sbase, unsigned voff) {
  asm volatile("global_load_dwordx4 %0, %1, %2" NTS : "=v"(dst) : "v"(voff), "s"(sbase) : "memory");
}
DI void gld16c(u32x4& dst, const char* sbase, unsigned voff) {
  asm volatile("global_load_dwordx4 %0, %1, %2 sc1" : "=v"(dst) : "v"(voff), "s"(sbase) : "memory");
}
DI void dma16(unsigned lds_dst, const char* sbase, unsigned voff) {
  unsigned keep;
  asm volatile("s_mov_b32 %0, m0\n\ts_mov_b32 m0, %3\n\ts_nop 0\n\tglobal_load_lds_dwordx4 %1, %2\n\ts_mov_b32 m0, %0"
               : "=&s"(keep) : "v"(voff), "s"(sbase), "s"(lds_dst) : "memory");
}
DI void dma16nt(unsigned lds_dst, const char* sbase, unsigned voff) {
  unsigned keep;
  asm volatile("s_mov_b32 %0, m0\n\ts_mov_b32 m0, %3\n\ts_nop 0\n\tglobal_load_lds_dwordx4 %1, %2" NTS "\n\ts_mov_b32 m0, %0"
               : "=&s"(keep) : "v"(voff), "s"(sbase), "s"(lds_dst) : "memory");
}
template <int N>
DI void wait_vm() { asm volatile("s_waitcnt vmcnt(%0)" ::"i"(N) : "memory"); }
template <int N>
DI void wait_vm8(u32x4 (&R)[8]) {
  asm volatile("s_waitcnt vmcnt(%8)"
               : "+v"(R[0]), "+v"(R[1]), "+v"(R[2]), "+v"(R[3]), "+v"(R[4]), "+v"(R[5]), "+v"(R[6]), "+v"(R[7])
               : "i"(N) : "memory");
}

template <int OFF>
DI void gld16o(u32x4& dst, const char* sbase, unsigned voff) {
  asm volatile("global_load_dwordx4 %0, %1, %2 offset:%3" NTS : "=v"(dst) : "v"(voff), "s"(sbase), "i"(OFF) : "memory");
}
DI void transpose_q(const u32x4 (&R)[8], u32x4 (&F)[8], int t) {
#pragma unroll
  for (int jj = 0; jj < 4; ++jj) {
    F[2 * t][jj] = __builtin_amdgcn_perm(R[2 * jj + 1][t], R[2 * jj][t], 0x05040100u);
    F[2 * t + 1][jj] = __builtin_amdgcn_perm(R[2 * jj + 1][t], R[2 * jj][t], 0x07060302u);
  }
}

DI void touch(unsigned& dst, const char* p) { asm volatile("global_load_dword %0, %1, off" : "+v"(dst) : "v"(p) : "memory"); }
__global__ __launch_bounds__(256, 1) void k1_attn(const uint16_t* __restrict__ x, const uint16_t* __restrict__ kc,
                                                  const uint16_t* __restrict__ vc, const uint16_t* __restrict__ wq,
                                                  const uint16_t* __restrict__ wo, char* __restrict__ ws,
                                                  uint16_t* __restrict__ out) {
  __shared__ __attribute__((aligned(16))) char lds[LDS_TOTAL];
  unsigned long long pc0;
  asm volatile("s_getpc_b64 %0" : "=s"(pc0));
  const int bid = blockIdx.x;
  const int g = bid & 7;
  const int idx = bid >> 3;
  const int p = (idx & 15) >> 1;
  const int wave = __builtin_amdgcn_readfirstlane(threadIdx.x >> 6);
  const int lane = threadIdx.x & 63;
  const int i = lane & 15, gq = lane >> 4;
  const int l8 = lane >> 3, e8 = lane & 7;
  const unsigned lds0 = (unsigned)(size_t)lds;
  if (idx < 16) {
    const int h = 2 * g + (idx & 1);
    const int half = wave & 1, kh = wave >> 1;
    auto gline = [&](int slot) {
      int r = slot + (ROT ? p : 0);
      r = r >= 7 ? r - 7 : r;
      r = r >= 7 ? r - 7 : r;
      return r;
    };
    auto line_of = [&](int t) { return 3 * kh + t; };
    auto ks_of = [&](int s) { return s < 6 ? 2 * gline(line_of(s >> 1)) + (s & 1) : 2 * gline(6) + kh; };
    const char* xs = (const char*)(x + (size_t)(2 * p) * SQ * DM);
    const unsigned xoff0 = (unsigned)(l8 * 896 + ((e8 ^ (l8 >> 1)) * 16));
    const unsigned xoff1 = (unsigned)(l8 * 896 + ((e8 ^ ((l8 >> 1) | 4)) * 16));
    auto xdma = [&](int L, int rg) { dma16(lds0 + LDS_X + L * 12288 + rg * 1024, xs + (size_t)rg * 8 * 896 + gline(L) * 128, (rg & 1) ? xoff1 : xoff0); };
    const char* kbase = (const char*)(kc + (size_t)((2 * p + (wave >> 1)) * HKV + g) * SK * DH) + (wave & 1) * 12288;
    auto kdma = [&](int n) {
      const int row = (wave * 12 + n) * 2 + (lane >> 5);
      dma16nt(lds0 + LDS_K + (wave * 12 + n) * 1024, kbase + n * 1024, (unsigned)((lane >> 5) * 512 + (((lane & 31) ^ swap02(row & 15)) * 16)));
    };
    unsigned woff[8];
#pragma unroll
    for (int j = 0; j < 8; ++j) woff[j] = (unsigned)((8 * gq + j) * (HQ * DH) + 8 * i) * 2u;
    const char* wbase = (const char*)(wq + h * DH + 128 * half);
    u32x4 R[3][8];
    auto wld = [&](int s, int j) { gld16(R[s % 3][j], wbase + (size_t)(32 * ks_of(s)) * (HQ * DH * 2), woff[j]); };
    auto xaddr = [&](int s, int qt) {
      const int L = s < 6 ? line_of(s >> 1) : 6;
      const int eo = s < 6 ? 4 * (s & 1) : 4 * kh;
      const int row = 16 * qt + i;
      return lds + LDS_X + L * 12288 + row * 128 + (((eo + gq) ^ ((row >> 1) & 7)) * 16);
    };
#pragma unroll
    for (int n = 0; n < 6; ++n) xdma(line_of(0), 6 * half + n);
#pragma unroll
    for (int j = 0; j < 8; ++j) wld(0, j);
    wait_vm<8>();
    __syncthreads();
#pragma unroll
    for (int j = 0; j < 8; ++j) wld(1, j);

    f32x4 acc[8][6];
#pragma unroll
    for (int c = 0; c < 8; ++c)
#pragma unroll
      for (int qt = 0; qt < 6; ++qt) acc[c][qt] = f32x4{0.f, 0.f, 0.f, 0.f};
    u32x4 F[2][8];
    u32x4 xb[2];
    xb[0] = *(const u32x4*)xaddr(0, 0);
    wait_vm8<8>(R[0]);
    transpose8(R[0], F[0]);
    SB();
#pragma unroll
    for (int s = 0; s < 7; ++s) {
#pragma unroll
      for (int qt = 0; qt < 6; ++qt) {
        const bool bar_after = (qt == 5) && (s == 1 || s == 3 || s == 5);
        if (qt < 5) xb[(qt + 1) & 1] = *(const u32x4*)xaddr(s, qt + 1);
        else if (s < 6 && !bar_after) xb[0] = *(const u32x4*)xaddr(s + 1, 0);
        if (qt == 2 && s < 6) {
          if (s == 0) wait_vm8<4>(R[1]);
          if (s == 1) wait_vm8<10>(R[2]);
          if (s == 2) wait_vm8<10>(R[0]);
          if (s == 3) wait_vm8<10>(R[1]);
          if (s == 4) wait_vm8<13>(R[2]);
          if (s == 5) wait_vm8<0>(R[0]);
        }
        if (qt == 0 && s == 4) { kdma(9); kdma(10); kdma(11); }
        if (qt < 4 && s < 5) { wld(s + 2, 2 * qt); wld(s + 2, 2 * qt + 1); }
        if (qt >= 4 && s == 0) { for (int n = 0; n < 3; ++n) xdma(line_of(1), 6 * half + 3 * (qt - 4) + n); }
        if (qt >= 4 && s == 1) { for (int n = 0; n < 3; ++n) xdma(line_of(2), 6 * half + 3 * (qt - 4) + n); }
        if (qt == 4 && s == 2) { for (int n = 0; n < 3; ++n) xdma(6, 3 * wave + n); }
        if (qt == 5 && s == 2) { kdma(0); kdma(1); kdma(2); }
        if (qt >= 4 && s == 3) { kdma(3 + 3 * (qt - 4)); kdma(4 + 3 * (qt - 4)); kdma(5 + 3 * (qt - 4)); }
        if (qt >= 2 && s < 6) transpose_q(R[(s + 1) % 3], F[(s + 1) & 1], qt - 2);
#pragma unroll
        for (int c = 0; c < 8; ++c) acc[c][qt] = MFMA(F[s & 1][c], xb[qt & 1], acc[c][qt]);
        if (!(s == 6)) SB();
        if (bar_after) {
          if (s == 1) wait_vm<14>();
          __syncthreads();
          xb[0] = *(const u32x4*)xaddr(s + 1, 0);
        }
      }
    }
#pragma unroll
    for (int bb = 0; bb < 2; ++bb) {
      u32x4 kf[3][4];
#pragma unroll
      for (int kt = 0; kt < 3; ++kt)
#pragma unroll
        for (int s = 0; s < 4; ++s) {
          const int row = 48 * bb + 16 * kt + i;
          kf[kt][s] = *(const u32x4*)(lds + LDS_K + row * 512 + (((16 * half + 4 * gq + s) ^ swap02(i)) * 16));
        }
#pragma unroll
      for (int qt = 0; qt < 3; ++qt) {
        const int q6 = 3 * bb + qt;
        f32x4 sacc[3];
#pragma unroll
        for (int kt = 0; kt < 3; ++kt) sacc[kt] = f32x4{0.f, 0.f, 0.f, 0.f};
#pragma unroll
        for (int s = 0; s < 4; ++s) {
          u32x4 qf = {pk2(acc[0][q6][s], acc[1][q6][s]), pk2(acc[2][q6][s], acc[3][q6][s]),
                      pk2(acc[4][q6][s], acc[5][q6][s]), pk2(acc[6][q6][s], acc[7][q6][s])};
#pragma unroll
          for (int kt = 0; kt < 3; ++kt) sacc[kt] = MFMA(kf[kt][s], qf, sacc[kt]);
        }
#pragma unroll
        for (int kt = 0; kt < 3; ++kt)
          *(f32x4*)(lds + LDS_X + (wave * 18 + (bb * 3 + qt) * 3 + kt) * 1024 + lane * 16) = sacc[kt];
      }
    }
    __syncthreads();
    {
      f32x4 s[2][3];
      const int u1 = wave < 2 ? wave + 4 : wave;
#pragma unroll
      for (int rep = 0; rep < 2; ++rep) {
        const int u = rep ? u1 : wave;
#pragma unroll
        for (int kt = 0; kt < 3; ++kt) {
          s[rep][kt] = *(const f32x4*)(lds + LDS_X + (0 * 18 + u * 3 + kt) * 1024 + lane * 16);
#pragma unroll
          for (int ww = 1; ww < 4; ++ww) s[rep][kt] += *(const f32x4*)(lds + LDS_X + (ww * 18 + u * 3 + kt) * 1024 + lane * 16);
        }
      }
      const float sc = 0.0625f * 1.44269504088896340736f;
#pragma unroll
      for (int rep = 0; rep < 2; ++rep) {
        float m = -INFINITY;
#pragma unroll
        for (int kt = 0; kt < 3; ++kt)
#pragma unroll
          for (int r = 0; r < 4; ++r) m = fmaxf(m, s[rep][kt][r]);
        m = red_max(m) * sc;
        float sum = 0.f;
#pragma unroll
        for (int kt = 0; kt < 3; ++kt)
#pragma unroll
          for (int r = 0; r < 4; ++r) {
            s[rep][kt][r] = __builtin_amdgcn_exp2f(__builtin_fmaf(s[rep][kt][r], sc, -m));
            sum += s[rep][kt][r];
          }
        sum = red_sum(sum);
        const float inv = __builtin_amdgcn_rcpf(sum);
#pragma unroll
        for (int kt = 0; kt < 3; ++kt) s[rep][kt] *= inv;
      }
#pragma unroll
      for (int rep = 0; rep < 2; ++rep) {
        if (rep == 0 || wave < 2) {
          const int u = rep ? u1 : wave;
          const int bb = u >= 3 ? 1 : 0, qt = u - 3 * bb;
          const int b = 2 * p + bb;
          st16_wt(ws + WS_PA + (size_t)((b * HQ + h) * 3 + qt) * 1024 + lane * 16, cat(pk4(s[rep][0]), pk4(s[rep][1])));
          st8_wt(ws + WS_PB + (size_t)((b * 8 + g) * 3 + qt) * 1024 + (((2 * (h & 1) + (gq >> 1)) << 4) | i) * 16 + (gq & 1) * 8, pk4(s[rep][2]));
        }
      }
    }
  } else {
    const int j = 4 * (idx & 1) + wave;
    const bool active = j < 7;
    auto line_of = [&](int t) { return ROT ? (t + p) & 3 : t; };
    const unsigned voff0 = (unsigned)(l8 * 512 + ((e8 ^ (l8 >> 1)) * 16));
    const unsigned voff1 = (unsigned)(l8 * 512 + ((e8 ^ ((l8 >> 1) | 4)) * 16));
    auto vdma = [&](int d, int n) {
      const int q = 3 * wave + n;
      const int t = 2 * (d & 1) + (q >= 6 ? 1 : 0);
      const int rgl = q >= 6 ? q - 6 : q;
      const int bb = d >> 1;
      const int L = line_of(t);
      const char* vs_ = (const char*)(vc + (size_t)((2 * p + bb) * HKV + g) * SK * DH);
      dma16nt(lds0 + LDS_V + L * 12288 + (6 * bb + rgl) * 1024, vs_ + (size_t)rgl * 8 * 512 + L * 128, (rgl & 1) ? voff1 : voff0);
    };
    if (!active) {
      unsigned sink = 0;
#pragma unroll
      for (int n = 0; n < 4; ++n) {
        const int ln = n * 64 + lane;
        touch(sink, (const char*)pc0 + (size_t)(ln < CODE_LINES ? ln : 0) * 128);
      }
#pragma unroll
      for (int d = 0; d < 4; ++d)
#pragma unroll
        for (int n = 0; n < 3; ++n) vdma(d, n);
      wait_vm<9>();
      asm volatile("" : "+v"(sink));
      __syncthreads();
      wait_vm<6>();
      __syncthreads();
      wait_vm<0>();
      __syncthreads();
    } else {
      const int np = 128 * j + 8 * i;
      const int hl = np >= DM ? 1 : 0;
      const int n0 = np - DM * hl;
      const unsigned woffa = (unsigned)((hl * DH + 8 * gq) * DM + n0) * 2u;
      const unsigned woffb = woffa + 4 * DM * 2;
      const char* wbase = (const char*)(wo + (size_t)(2 * g) * DH * DM);
      u32x4 R[3][8];
      auto wld = [&](int s, int jr) {
        const char* sb = wbase + (size_t)(32 * (2 * line_of(s >> 1) + (s & 1))) * (DM * 2);
        if (jr == 0) gld16o<0>(R[s % 3][0], sb, woffa);
        if (jr == 1) gld16o<896>(R[s % 3][1], sb, woffa);
        if (jr == 2) gld16o<1792>(R[s % 3][2], sb, woffa);
        if (jr == 3) gld16o<2688>(R[s % 3][3], sb, woffa);
        if (jr == 4) gld16o<0>(R[s % 3][4], sb, woffb);
        if (jr == 5) gld16o<896>(R[s % 3][5], sb, woffb);
        if (jr == 6) gld16o<1792>(R[s % 3][6], sb, woffb);
        if (jr == 7) gld16o<2688>(R[s % 3][7], sb, woffb);
      };
      auto vaddr = [&](int s, int mt) {
        const int row = 16 * mt + i;
        return lds + LDS_V + line_of(s >> 1) * 12288 + row * 128 + (((4 * (s & 1) + gq) ^ ((row >> 1) & 7)) * 16);
      };
      const int m = n0 >> 6, ip = (n0 >> 3) & 7;
      const int h = 2 * g + hl;
      char* va_base = ws + WS_VA + (size_t)(((2 * p * HQ + h) * 7 + m) * 4) * 1024 + ((gq << 4) | ip) * 16;
      char* vb_base = ws + WS_VB + (size_t)(((2 * p * 8 + g) * 7 + m) * 4) * 1024 + (((2 * hl + (gq >> 1)) << 4) | ((gq & 1) << 3) | ip) * 16;
      auto store_c2 = [&](f32x4 (&acc)[3][8], int bb, int c2) {
#pragma unroll
        for (int e = 0; e < 2; ++e)
          st16_wt(va_base + (size_t)bb * HQ * 28 * 1024 + c2 * 1024 + e * 128, cat(pk4(acc[0][2 * c2 + e]), pk4(acc[1][2 * c2 + e])));
        u32x2 a = pk4(acc[2][2 * c2]), bq = pk4(acc[2][2 * c2 + 1]);
        auto r0 = __builtin_amdgcn_permlane16_swap(a[0], bq[0], false, false);
        auto r1 = __builtin_amdgcn_permlane16_swap(a[1], bq[1], false, false);
        u32x4 t = {(unsigned)r0[0], (unsigned)r1[0], (unsigned)r0[1], (unsigned)r1[1]};
        st16_wt(vb_base + (size_t)bb * 8 * 28 * 1024 + c2 * 1024, t);
      };
#pragma unroll
      for (int n = 0; n < 3; ++n) vdma(0, n);
#pragma unroll
      for (int jr = 0; jr < 8; ++jr) wld(0, jr);
      wait_vm<8>();
      __syncthreads();
#pragma unroll
      for (int jr = 0; jr < 8; ++jr) wld(1, jr);
      f32x4 acc0[3][8], acc1[3][8];
#pragma unroll
      for (int mt = 0; mt < 3; ++mt)
#pragma unroll
        for (int c = 0; c < 8; ++c) { acc0[mt][c] = f32x4{0.f, 0.f, 0.f, 0.f}; acc1[mt][c] = f32x4{0.f, 0.f, 0.f, 0.f}; }
      u32x4 F[8][8];
      u32x4 va[2];
      va[0] = *(const u32x4*)vaddr(0, 0);
      wait_vm8<8>(R[0]);
      transpose8(R[0], F[0]);
      SB();
#pragma unroll
      for (int s = 0; s < 8; ++s) {
#pragma unroll
        for (int mt = 0; mt < 3; ++mt) {
          const int gidx = 3 * s + mt;
          const bool bar_after = (mt == 2) && (s == 3 || s == 7);
          if (mt < 2) va[(gidx + 1) & 1] = *(const u32x4*)vaddr(s, mt + 1);
          else if (!bar_after) va[(gidx + 1) & 1] = *(const u32x4*)vaddr(s + 1, 0);
          if (mt == 1 && s < 7) {
            if (s == 0) wait_vm8<3>(R[1]);
            if (s == 1) wait_vm8<6>(R[2]);
            if (s == 2) wait_vm8<6>(R[0]);
            if (s == 3) wait_vm8<6>(R[1]);
            if (s == 4) wait_vm8<3>(R[2]);
            if (s == 5) wait_vm8<3>(R[0]);
            if (s == 6) wait_vm8<0>(R[1]);
          }
          if (s < 6) {
            if (mt == 0) { wld(s + 2, 0); wld(s + 2, 1); wld(s + 2, 2); }
            if (mt == 1) { wld(s + 2, 3); wld(s + 2, 4); wld(s + 2, 5); }
            if (mt == 2) { wld(s + 2, 6); wld(s + 2, 7); }
          }
          if (mt == 2 && s < 3) { for (int n = 0; n < 3; ++n) vdma(s + 1, n); }
          if (mt >= 1 && s < 7) { transpose_q(R[(s + 1) % 3], F[s + 1], 2 * (mt - 1)); transpose_q(R[(s + 1) % 3], F[s + 1], 2 * (mt - 1) + 1); }
#pragma unroll
          for (int c = 0; c < 8; ++c) acc0[mt][c] = MFMA(va[gidx & 1], F[s][c], acc0[mt][c]);
          SB();
          if (bar_after) {
            __syncthreads();
            if (s != 7) va[(gidx + 1) & 1] = *(const u32x4*)vaddr(s + 1, 0);
          }
        }
        }
      {
        u32x4 vb[3][3];
#pragma unroll
        for (int mt = 0; mt < 3; ++mt) { vb[0][mt] = *(const u32x4*)vaddr(0, 3 + mt); vb[1][mt] = *(const u32x4*)vaddr(1, 3 + mt); }
#pragma unroll
        for (int c2 = 0; c2 < 4; ++c2) {
#pragma unroll
          for (int s = 0; s < 8; ++s) {
            const int n = c2 * 8 + s;
            if (n + 2 < 32) {
#pragma unroll
              for (int mt = 0; mt < 3; ++mt) vb[(n + 2) % 3][mt] = *(const u32x4*)vaddr((s + 2) & 7, 3 + mt);
            }
            if (s == 0) store_c2(acc0, 0, c2);
            if (s == 4 && c2 > 0) store_c2(acc1, 1, c2 - 1);
#pragma unroll
            for (int mt = 0; mt < 3; ++mt)
#pragma unroll
              for (int e = 0; e < 2; ++e) acc1[mt][2 * c2 + e] = MFMA(vb[n % 3][mt], F[s][2 * c2 + e], acc1[mt][2 * c2 + e]);
            SB();
          }
        }
      }
      store_c2(acc1, 1, 3);
    }
  }

  asm volatile("s_waitcnt vmcnt(0)" ::: "memory");
  __syncthreads();
  {
    const int prod = idx < 16 ? 2 * g + (idx & 1) : 16 + 2 * g + (idx & 1);
    if (wave == 0 && lane < 14) {
      unsigned long long* f = (unsigned long long*)(ws + WS_FL) + ((size_t)(p * 14 + lane) * 32 + prod);
      __hip_atomic_store(f, MAGIC, __ATOMIC_RELAXED, __HIP_MEMORY_SCOPE_AGENT);
    }
  }
  if (idx >= 16 && idx < 30) {
    const int ib = idx - 16;
    const int tb = g + 8 * (ib / 7), tm = ib % 7;
    const int tp = tb >> 1;
    unsigned long long* fl = (unsigned long long*)(ws + WS_FL) + (size_t)(tp * 14 + (tb & 1) * 7 + tm) * 32;
    auto wait_flags = [&](int first) {
      for (unsigned spin = 0; spin < (1u << 22); ++spin) {
        unsigned long long v = MAGIC;
        if (lane < 4) v = __hip_atomic_load(fl + first + lane, __ATOMIC_RELAXED, __HIP_MEMORY_SCOPE_AGENT);
        if (__all(v == MAGIC)) break;
        __builtin_amdgcn_s_sleep(1);
      }
      if (lane < 4) __hip_atomic_store(fl + first + lane, 0ull, __ATOMIC_RELAXED, __HIP_MEMORY_SCOPE_AGENT);
    };
    u32x4 pf[2][3][3], wf[2][3][4];
    const unsigned lo = lane * 16;
    wait_flags(16 + 4 * wave);
#pragma unroll
    for (int hp2 = 0; hp2 < 2; ++hp2) {
      const int hp = 2 * wave + hp2;
#pragma unroll
      for (int c2 = 0; c2 < 4; ++c2) {
        gld16c(wf[hp2][0][c2], ws + WS_VA + (size_t)(((tb * HQ + 2 * hp) * 7 + tm) * 4 + c2) * 1024, lo);
        gld16c(wf[hp2][1][c2], ws + WS_VA + (size_t)(((tb * HQ + 2 * hp + 1) * 7 + tm) * 4 + c2) * 1024, lo);
        gld16c(wf[hp2][2][c2], ws + WS_VB + (size_t)(((tb * 8 + hp) * 7 + tm) * 4 + c2) * 1024, lo);
      }
    }
    asm volatile("s_waitcnt vmcnt(0)"
                 : "+v"(wf[0][0][0]), "+v"(wf[0][0][1]), "+v"(wf[0][0][2]), "+v"(wf[0][0][3]), "+v"(wf[0][1][0]), "+v"(wf[0][1][1]),
                   "+v"(wf[0][1][2]), "+v"(wf[0][1][3]), "+v"(wf[0][2][0]), "+v"(wf[0][2][1]), "+v"(wf[0][2][2]), "+v"(wf[0][2][3]),
                   "+v"(wf[1][0][0]), "+v"(wf[1][0][1]), "+v"(wf[1][0][2]), "+v"(wf[1][0][3]), "+v"(wf[1][1][0]), "+v"(wf[1][1][1]),
                   "+v"(wf[1][1][2]), "+v"(wf[1][1][3]), "+v"(wf[1][2][0]), "+v"(wf[1][2][1]), "+v"(wf[1][2][2]), "+v"(wf[1][2][3])
                 :: "memory");
    wait_flags(4 * wave);
#pragma unroll
    for (int qt = 0; qt < 3; ++qt)
#pragma unroll
      for (int hp2 = 0; hp2 < 2; ++hp2) {
        const int hp = 2 * wave + hp2;
        gld16c(pf[hp2][0][qt], ws + WS_PA + (size_t)((tb * HQ + 2 * hp) * 3 + qt) * 1024, lo);
        gld16c(pf[hp2][1][qt], ws + WS_PA + (size_t)((tb * HQ + 2 * hp + 1) * 3 + qt) * 1024, lo);
        gld16c(pf[hp2][2][qt], ws + WS_PB + (size_t)((tb * 8 + hp) * 3 + qt) * 1024, lo);
      }
#pragma unroll
    for (int qt = 0; qt < 3; ++qt) {
      asm volatile("s_waitcnt vmcnt(%6)"
                   : "+v"(pf[0][0][qt]), "+v"(pf[0][1][qt]), "+v"(pf[0][2][qt]), "+v"(pf[1][0][qt]), "+v"(pf[1][1][qt]), "+v"(pf[1][2][qt])
                   : "i"(12 - 6 * qt));
#pragma unroll
      for (int c2 = 0; c2 < 4; ++c2) {
        f32x4 a = f32x4{0.f, 0.f, 0.f, 0.f};
#pragma unroll
        for (int hp2 = 0; hp2 < 2; ++hp2)
#pragma unroll
          for (int u = 0; u < 3; ++u) a = MFMA(pf[hp2][u][qt], wf[hp2][u][c2], a);
        *(f32x4*)(lds + ((wave * 3 + qt) * 4 + c2) * 1024 + lane * 16) = a;
      }
    }
    __syncthreads();
    const int tid = threadIdx.x;
#pragma unroll
    for (int rep = 0; rep < 2; ++rep) {
      const int t = tid + 256 * rep;
      if (t < 384) {
        const int q = t >> 3, ip = t & 7;
        const int qt = q >> 4, g4 = (q >> 2) & 3, r = q & 3;
        float v[8];
#pragma unroll
        for (int c = 0; c < 8; ++c) v[c] = 0.f;
#pragma unroll
        for (int ww = 0; ww < 4; ++ww)
#pragma unroll
          for (int c = 0; c < 8; ++c)
            v[c] += *(const float*)(lds + ((ww * 3 + qt) * 4 + (c >> 1)) * 1024 + ((g4 << 4) | ((c & 1) << 3) | ip) * 16 + r * 4);
        u32x4 o = {pk2(v[0], v[1]), pk2(v[2], v[3]), pk2(v[4], v[5]), pk2(v[6], v[7])};
        *(u32x4*)(out + (size_t)(tb * SQ + q) * DM + 64 * tm + 8 * ip) = o;
      }
    }
  }
}

extern "C" void kernel_launch(void* const* d_in, const int* in_sizes, int n_in, void* d_out, int out_size, void* d_ws,
                              size_t ws_size, hipStream_t stream) {
  const uint16_t* x = (const uint16_t*)d_in[0];
  const uint16_t* kc = (const uint16_t*)d_in[1];
  const uint16_t* vc = (const uint16_t*)d_in[2];
  const uint16_t* wq = (const uint16_t*)d_in[3];
  const uint16_t* wo = (const uint16_t*)d_in[4];
  k1_attn<<<256, 256, 0, stream>>>(x, kc, vc, wq, wo, (char*)d_ws, (uint16_t*)d_out);
}
```

```cpp
#include <hip/hip_runtime.h>
#include <hip/hip_bf16.h>
#include <stdint.h>

typedef __attribute__((ext_vector_type(8))) __bf16 bf16x8;
typedef __attribute__((ext_vector_type(2))) __bf16 bf16x2;
typedef __attribute__((ext_vector_type(4))) float f32x4;
typedef __attribute__((ext_vector_type(2))) float f32x2;
typedef __attribute__((ext_vector_type(4))) unsigned u32x4;
typedef __attribute__((ext_vector_type(2))) unsigned u32x2;

#define DI __device__ __forceinline__
#define MFMA(a, b, c) __builtin_amdgcn_mfma_f32_16x16x32_bf16(__builtin_bit_cast(bf16x8, (a)), __builtin_bit_cast(bf16x8, (b)), (c), 0, 0, 0)
#define SB() __builtin_amdgcn_sched_barrier(0)
#define NTS ""

constexpr int NB = 16, SQ = 48, DM = 448, HQ = 16, HKV = 8, SK = 48, DH = 256;

constexpr size_t WS_PA = 0;
constexpr size_t WS_PB = WS_PA + (size_t)NB * HQ * 3 * 1024;
constexpr size_t WS_VA = WS_PB + (size_t)NB * 8 * 3 * 1024;
constexpr size_t WS_VB = WS_VA + (size_t)NB * HQ * 28 * 1024;
constexpr size_t WS_FL = WS_VB + (size_t)NB * 8 * 28 * 1024;
constexpr unsigned long long MAGIC = 0x9E3779B97F4A7C15ull;

DI unsigned pk2(float a, float b) {
  f32x2 v = {a, b};
  bf16x2 r = __builtin_convertvector(v, bf16x2);
  return __builtin_bit_cast(unsigned, r);
}
DI u32x2 pk4(f32x4 v) {
  u32x2 r = {pk2(v[0], v[1]), pk2(v[2], v[3])};
  return r;
}
DI u32x4 cat(u32x2 a, u32x2 b) {
  u32x4 r = {a[0], a[1], b[0], b[1]};
  return r;
}
DI int swap02(int i) { return (i & 10) | ((i & 1) << 2) | ((i >> 2) & 1); }
DI void st16_wt(char* p, u32x4 v) { asm volatile("global_store_dwordx4 %0, %1, off sc1\n\ts_nop 1" ::"v"(p), "v"(v) : "memory"); }
DI void st8_wt(char* p, u32x2 v) { asm volatile("global_store_dwordx2 %0, %1, off sc1\n\ts_nop 1" ::"v"(p), "v"(v) : "memory"); }

DI float red_max(float m) {
  unsigned u = __builtin_bit_cast(unsigned, m);
  auto r = __builtin_amdgcn_permlane32_swap(u, u, false, false);
  m = fmaxf(__builtin_bit_cast(float, (unsigned)r[0]), __builtin_bit_cast(float, (unsigned)r[1]));
  u = __builtin_bit_cast(unsigned, m);
  auto r2 = __builtin_amdgcn_permlane16_swap(u, u, false, false);
  return fmaxf(__builtin_bit_cast(float, (unsigned)r2[0]), __builtin_bit_cast(float, (unsigned)r2[1]));
}
DI float red_sum(float m) {
  unsigned u = __builtin_bit_cast(unsigned, m);
  auto r = __builtin_amdgcn_permlane32_swap(u, u, false, false);
  m = __builtin_bit_cast(float, (unsigned)r[0]) + __builtin_bit_cast(float, (unsigned)r[1]);
  u = __builtin_bit_cast(unsigned, m);
  auto r2 = __builtin_amdgcn_permlane16_swap(u, u, false, false);
  return __builtin_bit_cast(float, (unsigned)r2[0]) + __builtin_bit_cast(float, (unsigned)r2[1]);
}

DI void transpose8(const u32x4 (&R)[8], u32x4 (&F)[8]) {
#pragma unroll
  for (int t = 0; t < 4; ++t)
#pragma unroll
    for (int jj = 0; jj < 4; ++jj) {
      F[2 * t][jj] = __builtin_amdgcn_perm(R[2 * jj + 1][t], R[2 * jj][t], 0x05040100u);
      F[2 * t + 1][jj] = __builtin_amdgcn_perm(R[2 * jj + 1][t], R[2 * jj][t], 0x07060302u);
    }
}

constexpr bool ROT = true;
constexpr int CODE_LINES = 208;
constexpr int LDS_X = 0;
constexpr int LDS_K = 96 * 896;
constexpr int LDS_V = 0;
constexpr int LDS_TOTAL = LDS_K + 96 * 512;

DI void gld16(u32x4& dst, const char* sbase, unsigned voff) {
  asm volatile("global_load_dwordx4 %0, %1, %2" NTS : "=v"(dst) : "v"(voff), "s"(sbase) : "memory");
}
DI void gld16c(u32x4& dst, const char* sbase, unsigned voff) {
  asm volatile("global_load_dwordx4 %0, %1, %2 sc1" : "=v"(dst) : "v"(voff), "s"(sbase) : "memory");
}
DI void dma16(unsigned lds_dst, const char* sbase, unsigned voff) {
  unsigned keep;
  asm volatile("s_mov_b32 %0, m0\n\ts_mov_b32 m0, %3\n\ts_nop 0\n\tglobal_load_lds_dwordx4 %1, %2\n\ts_mov_b32 m0, %0"
               : "=&s"(keep) : "v"(voff), "s"(sbase), "s"(lds_dst) : "memory");
}
DI void dma16nt(unsigned lds_dst, const char* sbase, unsigned voff) {
  unsigned keep;
  asm volatile("s_mov_b32 %0, m0\n\ts_mov_b32 m0, %3\n\ts_nop 0\n\tglobal_load_lds_dwordx4 %1, %2" NTS "\n\ts_mov_b32 m0, %0"
               : "=&s"(keep) : "v"(voff), "s"(sbase), "s"(lds_dst) : "memory");
}
template <int N>
DI void wait_vm() { asm volatile("s_waitcnt vmcnt(%0)" ::"i"(N) : "memory"); }
template <int N>
DI void wait_vm8(u32x4 (&R)[8]) {
  asm volatile("s_waitcnt vmcnt(%8)"
               : "+v"(R[0]), "+v"(R[1]), "+v"(R[2]), "+v"(R[3]), "+v"(R[4]), "+v"(R[5]), "+v"(R[6]), "+v"(R[7])
               : "i"(N) : "memory");
}

template <int OFF>
DI void gld16o(u32x4& dst, const char* sbase, unsigned voff) {
  asm volatile("global_load_dwordx4 %0, %1, %2 offset:%3" NTS : "=v"(dst) : "v"(voff), "s"(sbase), "i"(OFF) : "memory");
}
DI void transpose_q(const u32x4 (&R)[8], u32x4 (&F)[8], int t) {
#pragma unroll
  for (int jj = 0; jj < 4; ++jj) {
    F[2 * t][jj] = __builtin_amdgcn_perm(R[2 * jj + 1][t], R[2 * jj][t], 0x05040100u);
    F[2 * t + 1][jj] = __builtin_amdgcn_perm(R[2 * jj + 1][t], R[2 * jj][t], 0x07060302u);
  }
}

DI void touch(unsigned& dst, const char* p) { asm volatile("global_load_dword %0, %1, off" : "+v"(dst) : "v"(p) : "memory"); }
__global__ __launch_bounds__(256, 1) void k1_attn(const uint16_t* __restrict__ x, const uint16_t* __restrict__ kc,
                                                  const uint16_t* __restrict__ vc, const uint16_t* __restrict__ wq,
                                                  const uint16_t* __restrict__ wo, char* __restrict__ ws,
                                                  uint16_t* __restrict__ out) {
  __shared__ __attribute__((aligned(16))) char lds[LDS_TOTAL];
  unsigned long long pc0;
  asm volatile("s_getpc_b64 %0" : "=s"(pc0));
  const int bid = blockIdx.x;
  const int g = bid & 7;
  const int idx = bid >> 3;
  const int p = (idx & 15) >> 1;
  const int wave = __builtin_amdgcn_readfirstlane(threadIdx.x >> 6);
  const int lane = threadIdx.x & 63;
  const int i = lane & 15, gq = lane >> 4;
  const int l8 = lane >> 3, e8 = lane & 7;
  const unsigned lds0 = (unsigned)(size_t)lds;
  if (idx < 16) {
    const int h = 2 * g + (idx & 1);
    const int half = wave & 1, kh = wave >> 1;
    auto gline = [&](int slot) {
      int r = slot + (ROT ? p : 0);
      r = r >= 7 ? r - 7 : r;
      r = r >= 7 ? r - 7 : r;
      return r;
    };
    auto line_of = [&](int t) { return 3 * kh + t; };
    auto ks_of = [&](int s) { return s < 6 ? 2 * gline(line_of(s >> 1)) + (s & 1) : 2 * gline(6) + kh; };
    const char* xs = (const char*)(x + (size_t)(2 * p) * SQ * DM);
    const unsigned xoff0 = (unsigned)(l8 * 896 + ((e8 ^ (l8 >> 1)) * 16));
    const unsigned xoff1 = (unsigned)(l8 * 896 + ((e8 ^ ((l8 >> 1) | 4)) * 16));
    auto xdma = [&](int L, int rg) { dma16(lds0 + LDS_X + L * 12288 + rg * 1024, xs + (size_t)rg * 8 * 896 + gline(L) * 128, (rg & 1) ? xoff1 : xoff0); };
    const char* kbase = (const char*)(kc + (size_t)((2 * p + (wave >> 1)) * HKV + g) * SK * DH) + (wave & 1) * 12288;
    auto kdma = [&](int n) {
      const int row = (wave * 12 + n) * 2 + (lane >> 5);
      dma16nt(lds0 + LDS_K + (wave * 12 + n) * 1024, kbase + n * 1024, (unsigned)((lane >> 5) * 512 + (((lane & 31) ^ swap02(row & 15)) * 16)));
    };
    unsigned woff[8];
#pragma unroll
    for (int j = 0; j < 8; ++j) woff[j] = (unsigned)((8 * gq + j) * (HQ * DH) + 8 * i) * 2u;
    const char* wbase = (const char*)(wq + h * DH + 128 * half);
    u32x4 R[3][8];
    auto wld = [&](int s, int j) { gld16(R[s % 3][j], wbase + (size_t)(32 * ks_of(s)) * (HQ * DH * 2), woff[j]); };
    auto xaddr = [&](int s, int qt) {
      const int L = s < 6 ? line_of(s >> 1) : 6;
      const int eo = s < 6 ? 4 * (s & 1) : 4 * kh;
      const int row = 16 * qt + i;
      return lds + LDS_X + L * 12288 + row * 128 + (((eo + gq) ^ ((row >> 1) & 7)) * 16);
    };
#pragma unroll
    for (int n = 0; n < 6; ++n) xdma(line_of(0), 6 * half + n);
#pragma unroll
    for (int j = 0; j < 8; ++j) wld(0, j);
    wait_vm<8>();
    __syncthreads();
#pragma unroll
    for (int j = 0; j < 8; ++j) wld(1, j);

    f32x4 acc[8][6];
#pragma unroll
    for (int c = 0; c < 8; ++c)
#pragma unroll
      for (int qt = 0; qt < 6; ++qt) acc[c][qt] = f32x4{0.f, 0.f, 0.f, 0.f};
    u32x4 F[2][8];
    u32x4 xb[2];
    xb[0] = *(const u32x4*)xaddr(0, 0);
    wait_vm8<8>(R[0]);
    transpose8(R[0], F[0]);
    SB();
#pragma unroll
    for (int s = 0; s < 7; ++s) {
#pragma unroll
      for (int qt = 0; qt < 6; ++qt) {
        const bool bar_after = (qt == 5) && (s == 1 || s == 3 || s == 5);
        if (qt < 5) xb[(qt + 1) & 1] = *(const u32x4*)xaddr(s, qt + 1);
        else if (s < 6 && !bar_after) xb[0] = *(const u32x4*)xaddr(s + 1, 0);
        if (qt == 2 && s < 6) {
          if (s == 0) wait_vm8<4>(R[1]);
          if (s == 1) wait_vm8<10>(R[2]);
          if (s == 2) wait_vm8<10>(R[0]);
          if (s == 3) wait_vm8<10>(R[1]);
          if (s == 4) wait_vm8<13>(R[2]);
          if (s == 5) wait_vm8<0>(R[0]);
        }
        if (qt == 0 && s == 4) { kdma(9); kdma(10); kdma(11); }
        if (qt < 4 && s < 5) { wld(s + 2, 2 * qt); wld(s + 2, 2 * qt + 1); }
        if (qt >= 4 && s == 0) { for (int n = 0; n < 3; ++n) xdma(line_of(1), 6 * half + 3 * (qt - 4) + n); }
        if (qt >= 4 && s == 1) { for (int n = 0; n < 3; ++n) xdma(line_of(2), 6 * half + 3 * (qt - 4) + n); }
        if (qt == 4 && s == 2) { for (int n = 0; n < 3; ++n) xdma(6, 3 * wave + n); }
        if (qt == 5 && s == 2) { kdma(0); kdma(1); kdma(2); }
        if (qt >= 4 && s == 3) { kdma(3 + 3 * (qt - 4)); kdma(4 + 3 * (qt - 4)); kdma(5 + 3 * (qt - 4)); }
        if (qt >= 2 && s < 6) transpose_q(R[(s + 1) % 3], F[(s + 1) & 1], qt - 2);
#pragma unroll
        for (int c = 0; c < 8; ++c) acc[c][qt] = MFMA(F[s & 1][c], xb[qt & 1], acc[c][qt]);
        if (!(s == 6)) SB();
        if (bar_after) {
          if (s == 1) wait_vm<14>();
          __syncthreads();
          xb[0] = *(const u32x4*)xaddr(s + 1, 0);
        }
      }
    }
#pragma unroll
    for (int bb = 0; bb < 2; ++bb) {
      u32x4 kf[3][4];
#pragma unroll
      for (int kt = 0; kt < 3; ++kt)
#pragma unroll
        for (int s = 0; s < 4; ++s) {
          const int row = 48 * bb + 16 * kt + i;
          kf[kt][s] = *(const u32x4*)(lds + LDS_K + row * 512 + (((16 * half + 4 * gq + s) ^ swap02(i)) * 16));
        }
#pragma unroll
      for (int qt = 0; qt < 3; ++qt) {
        const int q6 = 3 * bb + qt;
        f32x4 sacc[3];
#pragma unroll
        for (int kt = 0; kt < 3; ++kt) sacc[kt] = f32x4{0.f, 0.f, 0.f, 0.f};
#pragma unroll
        for (int s = 0; s < 4; ++s) {
          u32x4 qf = {pk2(acc[0][q6][s], acc[1][q6][s]), pk2(acc[2][q6][s], acc[3][q6][s]),
                      pk2(acc[4][q6][s], acc[5][q6][s]), pk2(acc[6][q6][s], acc[7][q6][s])};
#pragma unroll
          for (int kt = 0; kt < 3; ++kt) sacc[kt] = MFMA(kf[kt][s], qf, sacc[kt]);
        }
#pragma unroll
        for (int kt = 0; kt < 3; ++kt)
          *(f32x4*)(lds + LDS_X + (wave * 18 + (bb * 3 + qt) * 3 + kt) * 1024 + lane * 16) = sacc[kt];
      }
    }
    __syncthreads();
    {
      f32x4 s[2][3];
      const int u1 = wave < 2 ? wave + 4 : wave;
#pragma unroll
      for (int rep = 0; rep < 2; ++rep) {
        const int u = rep ? u1 : wave;
#pragma unroll
        for (int kt = 0; kt < 3; ++kt) {
          s[rep][kt] = *(const f32x4*)(lds + LDS_X + (0 * 18 + u * 3 + kt) * 1024 + lane * 16);
#pragma unroll
          for (int ww = 1; ww < 4; ++ww) s[rep][kt] += *(const f32x4*)(lds + LDS_X + (ww * 18 + u * 3 + kt) * 1024 + lane * 16);
        }
      }
      const float sc = 0.0625f * 1.44269504088896340736f;
#pragma unroll
      for (int rep = 0; rep < 2; ++rep) {
        float m = -INFINITY;
#pragma unroll
        for (int kt = 0; kt < 3; ++kt)
#pragma unroll
          for (int r = 0; r < 4; ++r) m = fmaxf(m, s[rep][kt][r]);
        m = red_max(m) * sc;
        float sum = 0.f;
#pragma unroll
        for (int kt = 0; kt < 3; ++kt)
#pragma unroll
          for (int r = 0; r < 4; ++r) {
            s[rep][kt][r] = __builtin_amdgcn_exp2f(__builtin_fmaf(s[rep][kt][r], sc, -m));
            sum += s[rep][kt][r];
          }
        sum = red_sum(sum);
        const float inv = __builtin_amdgcn_rcpf(sum);
#pragma unroll
        for (int kt = 0; kt < 3; ++kt) s[rep][kt] *= inv;
      }
#pragma unroll
      for (int rep = 0; rep < 2; ++rep) {
        if (rep == 0 || wave < 2) {
          const int u = rep ? u1 : wave;
          const int bb = u >= 3 ? 1 : 0, qt = u - 3 * bb;
          const int b = 2 * p + bb;
          st16_wt(ws + WS_PA + (size_t)((b * HQ + h) * 3 + qt) * 1024 + lane * 16, cat(pk4(s[rep][0]), pk4(s[rep][1])));
          st8_wt(ws + WS_PB + (size_t)((b * 8 + g) * 3 + qt) * 1024 + (((2 * (h & 1) + (gq >> 1)) << 4) | i) * 16 + (gq & 1) * 8, pk4(s[rep][2]));
        }
      }
    }
  } else {
    const int j = 4 * (idx & 1) + wave;
    const bool active = j < 7;
    auto line_of = [&](int t) { return ROT ? (t + p) & 3 : t; };
    const unsigned voff0 = (unsigned)(l8 * 512 + ((e8 ^ (l8 >> 1)) * 16));
    const unsigned voff1 = (unsigned)(l8 * 512 + ((e8 ^ ((l8 >> 1) | 4)) * 16));
    auto vdma = [&](int d, int n) {
      const int q = 3 * wave + n;
      const int t = 2 * (d & 1) + (q >= 6 ? 1 : 0);
      const int rgl = q >= 6 ? q - 6 : q;
      const int bb = d >> 1;
      const int L = line_of(t);
      const char* vs_ = (const char*)(vc + (size_t)((2 * p + bb) * HKV + g) * SK * DH);
      dma16nt(lds0 + LDS_V + L * 12288 + (6 * bb + rgl) * 1024, vs_ + (size_t)rgl * 8 * 512 + L * 128, (rgl & 1) ? voff1 : voff0);
    };
    if (!active) {
      unsigned sink = 0;
#pragma unroll
      for (int n = 0; n < 4; ++n) {
        const int ln = n * 64 + lane;
        touch(sink, (const char*)pc0 + (size_t)(ln < CODE_LINES ? ln : 0) * 128);
      }
#pragma unroll
      for (int d = 0; d < 4; ++d)
#pragma unroll
        for (int n = 0; n < 3; ++n) vdma(d, n);
      wait_vm<9>();
      asm volatile("" : "+v"(sink));
      __syncthreads();
      wait_vm<6>();
      __syncthreads();
      wait_vm<0>();
      __syncthreads();
    } else {
      const int np = 128 * j + 8 * i;
      const int hl = np >= DM ? 1 : 0;
      const int n0 = np - DM * hl;
      const unsigned woffa = (unsigned)((hl * DH + 8 * gq) * DM + n0) * 2u;
      const unsigned woffb = woffa + 4 * DM * 2;
      const char* wbase = (const char*)(wo + (size_t)(2 * g) * DH * DM);
      u32x4 R[3][8];
      auto wld = [&](int s, int jr) {
        const char* sb = wbase + (size_t)(32 * (2 * line_of(s >> 1) + (s & 1))) * (DM * 2);
        if (jr == 0) gld16o<0>(R[s % 3][0], sb, woffa);
        if (jr == 1) gld16o<896>(R[s % 3][1], sb, woffa);
        if (jr == 2) gld16o<1792>(R[s % 3][2], sb, woffa);
        if (jr == 3) gld16o<2688>(R[s % 3][3], sb, woffa);
        if (jr == 4) gld16o<0>(R[s % 3][4], sb, woffb);
        if (jr == 5) gld16o<896>(R[s % 3][5], sb, woffb);
        if (jr == 6) gld16o<1792>(R[s % 3][6], sb, woffb);
        if (jr == 7) gld16o<2688>(R[s % 3][7], sb, woffb);
      };
      auto vaddr = [&](int s, int mt) {
        const int row = 16 * mt + i;
        return lds + LDS_V + line_of(s >> 1) * 12288 + row * 128 + (((4 * (s & 1) + gq) ^ ((row >> 1) & 7)) * 16);
      };
      const int m = n0 >> 6, ip = (n0 >> 3) & 7;
      const int h = 2 * g + hl;
      char* va_base = ws + WS_VA + (size_t)(((2 * p * HQ + h) * 7 + m) * 4) * 1024 + ((gq << 4) | ip) * 16;
      char* vb_base = ws + WS_VB + (size_t)(((2 * p * 8 + g) * 7 + m) * 4) * 1024 + (((2 * hl + (gq >> 1)) << 4) | ((gq & 1) << 3) | ip) * 16;
      auto store_c2 = [&](f32x4 (&acc)[3][8], int bb, int c2) {
#pragma unroll
        for (int e = 0; e < 2; ++e)
          st16_wt(va_base + (size_t)bb * HQ * 28 * 1024 + c2 * 1024 + e * 128, cat(pk4(acc[0][2 * c2 + e]), pk4(acc[1][2 * c2 + e])));
        u32x2 a = pk4(acc[2][2 * c2]), bq = pk4(acc[2][2 * c2 + 1]);
        auto r0 = __builtin_amdgcn_permlane16_swap(a[0], bq[0], false, false);
        auto r1 = __builtin_amdgcn_permlane16_swap(a[1], bq[1], false, false);
        u32x4 t = {(unsigned)r0[0], (unsigned)r1[0], (unsigned)r0[1], (unsigned)r1[1]};
        st16_wt(vb_base + (size_t)bb * 8 * 28 * 1024 + c2 * 1024, t);
      };
#pragma unroll
      for (int n = 0; n < 3; ++n) vdma(0, n);
#pragma unroll
      for (int jr = 0; jr < 8; ++jr) wld(0, jr);
      wait_vm<8>();
      __syncthreads();
#pragma unroll
      for (int jr = 0; jr < 8; ++jr) wld(1, jr);
      f32x4 acc0[3][8], acc1[3][8];
#pragma unroll
      for (int mt = 0; mt < 3; ++mt)
#pragma unroll
        for (int c = 0; c < 8; ++c) { acc0[mt][c] = f32x4{0.f, 0.f, 0.f, 0.f}; acc1[mt][c] = f32x4{0.f, 0.f, 0.f, 0.f}; }
      u32x4 F[8][8];
      u32x4 va[2];
      va[0] = *(const u32x4*)vaddr(0, 0);
      wait_vm8<8>(R[0]);
      transpose8(R[0], F[0]);
      SB();
#pragma unroll
      for (int s = 0; s < 8; ++s) {
#pragma unroll
        for (int mt = 0; mt < 3; ++mt) {
          const int gidx = 3 * s + mt;
          const bool bar_after = (mt == 2) && (s == 3 || s == 7);
          if (mt < 2) va[(gidx + 1) & 1] = *(const u32x4*)vaddr(s, mt + 1);
          else if (!bar_after) va[(gidx + 1) & 1] = *(const u32x4*)vaddr(s + 1, 0);
          if (mt == 1 && s < 7) {
            if (s == 0) wait_vm8<3>(R[1]);
            if (s == 1) wait_vm8<6>(R[2]);
            if (s == 2) wait_vm8<3>(R[0]);
            if (s == 3) wait_vm8<3>(R[1]);
            if (s == 4) wait_vm8<3>(R[2]);
            if (s == 5) wait_vm8<3>(R[0]);
            if (s == 6) wait_vm8<3>(R[1]);
          }
          if (s < 6) {
            if (mt == 0) { wld(s + 2, 0); wld(s + 2, 1); wld(s + 2, 2); }
            if (mt == 1) { wld(s + 2, 3); wld(s + 2, 4); wld(s + 2, 5); }
            if (mt == 2) { wld(s + 2, 6); wld(s + 2, 7); }
          }
          if (mt == 2 && s == 0) { for (int n = 0; n < 3; ++n) vdma(1, n); }
          if (mt == 2 && (s == 5 || s == 6)) { for (int n = 0; n < 3; ++n) vdma(s - 3, n); }
          if (mt >= 1 && s < 7) { transpose_q(R[(s + 1) % 3], F[s + 1], 2 * (mt - 1)); transpose_q(R[(s + 1) % 3], F[s + 1], 2 * (mt - 1) + 1); }
#pragma unroll
          for (int c = 0; c < 8; ++c) acc0[mt][c] = MFMA(va[gidx & 1], F[s][c], acc0[mt][c]);
          SB();
          if (bar_after) {
            if (s == 7) wait_vm<0>();
            __syncthreads();
            if (s != 7) va[(gidx + 1) & 1] = *(const u32x4*)vaddr(s + 1, 0);
          }
        }
        }
      {
        u32x4 vb[3][3];
#pragma unroll
        for (int mt = 0; mt < 3; ++mt) { vb[0][mt] = *(const u32x4*)vaddr(0, 3 + mt); vb[1][mt] = *(const u32x4*)vaddr(1, 3 + mt); }
#pragma unroll
        for (int c2 = 0; c2 < 4; ++c2) {
#pragma unroll
          for (int s = 0; s < 8; ++s) {
            const int n = c2 * 8 + s;
            if (n + 2 < 32) {
#pragma unroll
              for (int mt = 0; mt < 3; ++mt) vb[(n + 2) % 3][mt] = *(const u32x4*)vaddr((s + 2) & 7, 3 + mt);
            }
            if (s == 0) store_c2(acc0, 0, c2);
            if (s == 4 && c2 > 0) store_c2(acc1, 1, c2 - 1);
#pragma unroll
            for (int mt = 0; mt < 3; ++mt)
#pragma unroll
              for (int e = 0; e < 2; ++e) acc1[mt][2 * c2 + e] = MFMA(vb[n % 3][mt], F[s][2 * c2 + e], acc1[mt][2 * c2 + e]);
            SB();
          }
        }
      }
      store_c2(acc1, 1, 3);
    }
  }

  asm volatile("s_waitcnt vmcnt(0)" ::: "memory");
  __syncthreads();
  {
    const int prod = idx < 16 ? 2 * g + (idx & 1) : 16 + 2 * g + (idx & 1);
    if (wave == 0 && lane < 14) {
      unsigned long long* f = (unsigned long long*)(ws + WS_FL) + ((size_t)(p * 14 + lane) * 32 + prod);
      __hip_atomic_store(f, MAGIC, __ATOMIC_RELAXED, __HIP_MEMORY_SCOPE_AGENT);
    }
  }
  if (idx >= 16 && idx < 30) {
    const int ib = idx - 16;
    const int tb = g + 8 * (ib / 7), tm = ib % 7;
    const int tp = tb >> 1;
    unsigned long long* fl = (unsigned long long*)(ws + WS_FL) + (size_t)(tp * 14 + (tb & 1) * 7 + tm) * 32;
    auto wait_flags = [&](int first, int cnt) {
      for (unsigned spin = 0; spin < (1u << 22); ++spin) {
        unsigned long long v = MAGIC;
        if (lane < cnt) v = __hip_atomic_load(fl + first + lane, __ATOMIC_RELAXED, __HIP_MEMORY_SCOPE_AGENT);
        if (__all(v == MAGIC)) break;
      }
      if (lane < cnt) __hip_atomic_store(fl + first + lane, 0ull, __ATOMIC_RELAXED, __HIP_MEMORY_SCOPE_AGENT);
    };
    u32x4 pf[2][3][3], wf[2][3][4];
    const unsigned lo = lane * 16;
    wait_flags(16 + 4 * wave, 4);
#pragma unroll
    for (int hp2 = 0; hp2 < 2; ++hp2) {
      const int hp = 2 * wave + hp2;
#pragma unroll
      for (int c2 = 0; c2 < 4; ++c2) {
        gld16c(wf[hp2][0][c2], ws + WS_VA + (size_t)(((tb * HQ + 2 * hp) * 7 + tm) * 4 + c2) * 1024, lo);
        gld16c(wf[hp2][1][c2], ws + WS_VA + (size_t)(((tb * HQ + 2 * hp + 1) * 7 + tm) * 4 + c2) * 1024, lo);
        gld16c(wf[hp2][2][c2], ws + WS_VB + (size_t)(((tb * 8 + hp) * 7 + tm) * 4 + c2) * 1024, lo);
      }
    }
    asm volatile("s_waitcnt vmcnt(0)"
                 : "+v"(wf[0][0][0]), "+v"(wf[0][0][1]), "+v"(wf[0][0][2]), "+v"(wf[0][0][3]), "+v"(wf[0][1][0]), "+v"(wf[0][1][1]),
                   "+v"(wf[0][1][2]), "+v"(wf[0][1][3]), "+v"(wf[0][2][0]), "+v"(wf[0][2][1]), "+v"(wf[0][2][2]), "+v"(wf[0][2][3]),
                   "+v"(wf[1][0][0]), "+v"(wf[1][0][1]), "+v"(wf[1][0][2]), "+v"(wf[1][0][3]), "+v"(wf[1][1][0]), "+v"(wf[1][1][1]),
                   "+v"(wf[1][1][2]), "+v"(wf[1][1][3]), "+v"(wf[1][2][0]), "+v"(wf[1][2][1]), "+v"(wf[1][2][2]), "+v"(wf[1][2][3])
                 :: "memory");
    wait_flags(4 * wave, 4);
#pragma unroll
    for (int qt = 0; qt < 3; ++qt)
#pragma unroll
      for (int hp2 = 0; hp2 < 2; ++hp2) {
        const int hp = 2 * wave + hp2;
        gld16c(pf[hp2][0][qt], ws + WS_PA + (size_t)((tb * HQ + 2 * hp) * 3 + qt) * 1024, lo);
        gld16c(pf[hp2][1][qt], ws + WS_PA + (size_t)((tb * HQ + 2 * hp + 1) * 3 + qt) * 1024, lo);
        gld16c(pf[hp2][2][qt], ws + WS_PB + (size_t)((tb * 8 + hp) * 3 + qt) * 1024, lo);
      }
#pragma unroll
    for (int qt = 0; qt < 3; ++qt) {
      asm volatile("s_waitcnt vmcnt(%6)"
                   : "+v"(pf[0][0][qt]), "+v"(pf[0][1][qt]), "+v"(pf[0][2][qt]), "+v"(pf[1][0][qt]), "+v"(pf[1][1][qt]), "+v"(pf[1][2][qt])
                   : "i"(12 - 6 * qt));
#pragma unroll
      for (int c2 = 0; c2 < 4; ++c2) {
        f32x4 a = f32x4{0.f, 0.f, 0.f, 0.f};
#pragma unroll
        for (int hp2 = 0; hp2 < 2; ++hp2)
#pragma unroll
          for (int u = 0; u < 3; ++u) a = MFMA(pf[hp2][u][qt], wf[hp2][u][c2], a);
        *(f32x4*)(lds + ((wave * 3 + qt) * 4 + c2) * 1024 + lane * 16) = a;
      }
    }
    __syncthreads();
    const int tid = threadIdx.x;
#pragma unroll
    for (int rep = 0; rep < 2; ++rep) {
      const int t = tid + 256 * rep;
      if (t < 384) {
        const int q = t >> 3, ip = t & 7;
        const int qt = q >> 4, g4 = (q >> 2) & 3, r = q & 3;
        float v[8];
#pragma unroll
        for (int c = 0; c < 8; ++c) v[c] = 0.f;
#pragma unroll
        for (int ww = 0; ww < 4; ++ww)
#pragma unroll
          for (int c = 0; c < 8; ++c)
            v[c] += *(const float*)(lds + ((ww * 3 + qt) * 4 + (c >> 1)) * 1024 + ((g4 << 4) | ((c & 1) << 3) | ip) * 16 + r * 4);
        u32x4 o = {pk2(v[0], v[1]), pk2(v[2], v[3]), pk2(v[4], v[5]), pk2(v[6], v[7])};
        *(u32x4*)(out + (size_t)(tb * SQ + q) * DM + 64 * tm + 8 * ip) = o;
      }
    }
  }
}

extern "C" void kernel_launch(void* const* d_in, const int* in_sizes, int n_in, void* d_out, int out_size, void* d_ws,
                              size_t ws_size, hipStream_t stream) {
  const uint16_t* x = (const uint16_t*)d_in[0];
  const uint16_t* kc = (const uint16_t*)d_in[1];
  const uint16_t* vc = (const uint16_t*)d_in[2];
  const uint16_t* wq = (const uint16_t*)d_in[3];
  const uint16_t* wo = (const uint16_t*)d_in[4];
  k1_attn<<<256, 256, 0, stream>>>(x, kc, vc, wq, wo, (char*)d_ws, (uint16_t*)d_out);
}
```

```cpp
#include <hip/hip_runtime.h>
#include <hip/hip_bf16.h>
#include <stdint.h>

typedef __attribute__((ext_vector_type(8))) __bf16 bf16x8;
typedef __attribute__((ext_vector_type(2))) __bf16 bf16x2;
typedef __attribute__((ext_vector_type(4))) float f32x4;
typedef __attribute__((ext_vector_type(2))) float f32x2;
typedef __attribute__((ext_vector_type(4))) unsigned u32x4;
typedef __attribute__((ext_vector_type(2))) unsigned u32x2;

#define DI __device__ __forceinline__
#define MFMA(a, b, c) __builtin_amdgcn_mfma_f32_16x16x32_bf16(__builtin_bit_cast(bf16x8, (a)), __builtin_bit_cast(bf16x8, (b)), (c), 0, 0, 0)
#define SB() __builtin_amdgcn_sched_barrier(0)
#define NTS ""

constexpr int NB = 16, SQ = 48, DM = 448, HQ = 16, HKV = 8, SK = 48, DH = 256;

constexpr size_t WS_PA = 0;
constexpr size_t WS_PB = WS_PA + (size_t)NB * HQ * 3 * 1024;
constexpr size_t WS_VA = WS_PB + (size_t)NB * 8 * 3 * 1024;
constexpr size_t WS_VB = WS_VA + (size_t)NB * HQ * 28 * 1024;
constexpr size_t WS_FL = WS_VB + (size_t)NB * 8 * 28 * 1024;
constexpr unsigned long long MAGIC = 0x9E3779B97F4A7C15ull;

DI unsigned pk2(float a, float b) {
  f32x2 v = {a, b};
  bf16x2 r = __builtin_convertvector(v, bf16x2);
  return __builtin_bit_cast(unsigned, r);
}
DI u32x2 pk4(f32x4 v) {
  u32x2 r = {pk2(v[0], v[1]), pk2(v[2], v[3])};
  return r;
}
DI u32x4 cat(u32x2 a, u32x2 b) {
  u32x4 r = {a[0], a[1], b[0], b[1]};
  return r;
}
DI int swap02(int i) { return (i & 10) | ((i & 1) << 2) | ((i >> 2) & 1); }
DI void st16_wt(char* p, u32x4 v) { asm volatile("global_store_dwordx4 %0, %1, off sc1\n\ts_nop 1" ::"v"(p), "v"(v) : "memory"); }
DI void st8_wt(char* p, u32x2 v) { asm volatile("global_store_dwordx2 %0, %1, off sc1\n\ts_nop 1" ::"v"(p), "v"(v) : "memory"); }

DI float red_max(float m) {
  unsigned u = __builtin_bit_cast(unsigned, m);
  auto r = __builtin_amdgcn_permlane32_swap(u, u, false, false);
  m = fmaxf(__builtin_bit_cast(float, (unsigned)r[0]), __builtin_bit_cast(float, (unsigned)r[1]));
  u = __builtin_bit_cast(unsigned, m);
  auto r2 = __builtin_amdgcn_permlane16_swap(u, u, false, false);
  return fmaxf(__builtin_bit_cast(float, (unsigned)r2[0]), __builtin_bit_cast(float, (unsigned)r2[1]));
}
DI float red_sum(float m) {
  unsigned u = __builtin_bit_cast(unsigned, m);
  auto r = __builtin_amdgcn_permlane32_swap(u, u, false, false);
  m = __builtin_bit_cast(float, (unsigned)r[0]) + __builtin_bit_cast(float, (unsigned)r[1]);
  u = __builtin_bit_cast(unsigned, m);
  auto r2 = __builtin_amdgcn_permlane16_swap(u, u, false, false);
  return __builtin_bit_cast(float, (unsigned)r2[0]) + __builtin_bit_cast(float, (unsigned)r2[1]);
}

DI void transpose8(const u32x4 (&R)[8], u32x4 (&F)[8]) {
#pragma unroll
  for (int t = 0; t < 4; ++t)
#pragma unroll
    for (int jj = 0; jj < 4; ++jj) {
      F[2 * t][jj] = __builtin_amdgcn_perm(R[2 * jj + 1][t], R[2 * jj][t], 0x05040100u);
      F[2 * t + 1][jj] = __builtin_amdgcn_perm(R[2 * jj + 1][t], R[2 * jj][t], 0x07060302u);
    }
}

constexpr bool ROT = true;
constexpr int CODE_LINES = 208;
constexpr int LDS_X = 0;
constexpr int LDS_K = 96 * 896;
constexpr int LDS_V = 0;
constexpr int LDS_TOTAL = LDS_K + 96 * 512;

DI void gld16(u32x4& dst, const char* sbase, unsigned voff) {
  asm volatile("global_load_dwordx4 %0, %1, %2" NTS : "=v"(dst) : "v"(voff), "s"(sbase) : "memory");
}
DI void gld16c(u32x4& dst, const char* sbase, unsigned voff) {
  asm volatile("global_load_dwordx4 %0, %1, %2 sc1" : "=v"(dst) : "v"(voff), "s"(sbase) : "memory");
}
DI void dma16(unsigned lds_dst, const char* sbase, unsigned voff) {
  unsigned keep;
  asm volatile("s_mov_b32 %0, m0\n\ts_mov_b32 m0, %3\n\ts_nop 0\n\tglobal_load_lds_dwordx4 %1, %2\n\ts_mov_b32 m0, %0"
               : "=&s"(keep) : "v"(voff), "s"(sbase), "s"(lds_dst) : "memory");
}
DI void dma16nt(unsigned lds_dst, const char* sbase, unsigned voff) {
  unsigned keep;
  asm volatile("s_mov_b32 %0, m0\n\ts_mov_b32 m0, %3\n\ts_nop 0\n\tglobal_load_lds_dwordx4 %1, %2" NTS "\n\ts_mov_b32 m0, %0"
               : "=&s"(keep) : "v"(voff), "s"(sbase), "s"(lds_dst) : "memory");
}
template <int N>
DI void wait_vm() { asm volatile("s_waitcnt vmcnt(%0)" ::"i"(N) : "memory"); }
template <int N>
DI void wait_vm8(u32x4 (&R)[8]) {
  asm volatile("s_waitcnt vmcnt(%8)"
               : "+v"(R[0]), "+v"(R[1]), "+v"(R[2]), "+v"(R[3]), "+v"(R[4]), "+v"(R[5]), "+v"(R[6]), "+v"(R[7])
               : "i"(N) : "memory");
}

template <int OFF>
DI void gld16o(u32x4& dst, const char* sbase, unsigned voff) {
  asm volatile("global_load_dwordx4 %0, %1, %2 offset:%3" NTS : "=v"(dst) : "v"(voff), "s"(sbase), "i"(OFF) : "memory");
}
DI void transpose_q(const u32x4 (&R)[8], u32x4 (&F)[8], int t) {
#pragma unroll
  for (int jj = 0; jj < 4; ++jj) {
    F[2 * t][jj] = __builtin_amdgcn_perm(R[2 * jj + 1][t], R[2 * jj][t], 0x05040100u);
    F[2 * t + 1][jj] = __builtin_amdgcn_perm(R[2 * jj + 1][t], R[2 * jj][t], 0x07060302u);
  }
}

DI void touch(unsigned& dst, const char* p) { asm volatile("global_load_dword %0, %1, off" : "+v"(dst) : "v"(p) : "memory"); }
__global__ __launch_bounds__(256, 1) void k1_attn(const uint16_t* __restrict__ x, const uint16_t* __restrict__ kc,
                                                  const uint16_t* __restrict__ vc, const uint16_t* __restrict__ wq,
                                                  const uint16_t* __restrict__ wo, char* __restrict__ ws,
                                                  uint16_t* __restrict__ out) {
  __shared__ __attribute__((aligned(16))) char lds[LDS_TOTAL];
  unsigned long long pc0;
  asm volatile("s_getpc_b64 %0" : "=s"(pc0));
  const int bid = blockIdx.x;
  const int g = bid & 7;
  const int idx = bid >> 3;
  const int p = (idx & 15) >> 1;
  const int wave = __builtin_amdgcn_readfirstlane(threadIdx.x >> 6);
  const int lane = threadIdx.x & 63;
  const int i = lane & 15, gq = lane >> 4;
  const int l8 = lane >> 3, e8 = lane & 7;
  const unsigned lds0 = (unsigned)(size_t)lds;
  if (idx < 16) {
    const int h = 2 * g + (idx & 1);
    const int half = wave & 1, kh = wave >> 1;
    auto gline = [&](int slot) {
      int r = slot + (ROT ? p : 0);
      r = r >= 7 ? r - 7 : r;
      r = r >= 7 ? r - 7 : r;
      return r;
    };
    auto line_of = [&](int t) { return 3 * kh + t; };
    auto ks_of = [&](int s) { return s < 6 ? 2 * gline(line_of(s >> 1)) + (s & 1) : 2 * gline(6) + kh; };
    const char* xs = (const char*)(x + (size_t)(2 * p) * SQ * DM);
    const unsigned xoff0 = (unsigned)(l8 * 896 + ((e8 ^ (l8 >> 1)) * 16));
    const unsigned xoff1 = (unsigned)(l8 * 896 + ((e8 ^ ((l8 >> 1) | 4)) * 16));
    auto xdma = [&](int L, int rg) { dma16(lds0 + LDS_X + L * 12288 + rg * 1024, xs + (size_t)rg * 8 * 896 + gline(L) * 128, (rg & 1) ? xoff1 : xoff0); };
    const char* kbase = (const char*)(kc + (size_t)((2 * p + (wave >> 1)) * HKV + g) * SK * DH) + (wave & 1) * 12288;
    auto kdma = [&](int n) {
      const int row = (wave * 12 + n) * 2 + (lane >> 5);
      dma16nt(lds0 + LDS_K + (wave * 12 + n) * 1024, kbase + n * 1024, (unsigned)((lane >> 5) * 512 + (((lane & 31) ^ swap02(row & 15)) * 16)));
    };
    unsigned woff[8];
#pragma unroll
    for (int j = 0; j < 8; ++j) woff[j] = (unsigned)((8 * gq + j) * (HQ * DH) + 8 * i) * 2u;
    const char* wbase = (const char*)(wq + h * DH + 128 * half);
    u32x4 R[3][8];
    auto wld = [&](int s, int j) { gld16(R[s % 3][j], wbase + (size_t)(32 * ks_of(s)) * (HQ * DH * 2), woff[j]); };
    auto xaddr = [&](int s, int qt) {
      const int L = s < 6 ? line_of(s >> 1) : 6;
      const int eo = s < 6 ? 4 * (s & 1) : 4 * kh;
      const int row = 16 * qt + i;
      return lds + LDS_X + L * 12288 + row * 128 + (((eo + gq) ^ ((row >> 1) & 7)) * 16);
    };
#pragma unroll
    for (int n = 0; n < 6; ++n) xdma(line_of(0), 6 * half + n);
#pragma unroll
    for (int j = 0; j < 8; ++j) wld(0, j);
    wait_vm<8>();
    __syncthreads();
#pragma unroll
    for (int j = 0; j < 8; ++j) wld(1, j);

    f32x4 acc[8][6];
#pragma unroll
    for (int c = 0; c < 8; ++c)
#pragma unroll
      for (int qt = 0; qt < 6; ++qt) acc[c][qt] = f32x4{0.f, 0.f, 0.f, 0.f};
    u32x4 F[2][8];
    u32x4 xb[2];
    xb[0] = *(const u32x4*)xaddr(0, 0);
    wait_vm8<8>(R[0]);
    transpose8(R[0], F[0]);
    SB();
#pragma unroll
    for (int s = 0; s < 7; ++s) {
#pragma unroll
      for (int qt = 0; qt < 6; ++qt) {
        const bool bar_after = (qt == 5) && (s == 1 || s == 3 || s == 5);
        if (qt < 5) xb[(qt + 1) & 1] = *(const u32x4*)xaddr(s, qt + 1);
        else if (s < 6 && !bar_after) xb[0] = *(const u32x4*)xaddr(s + 1, 0);
        if (qt == 2 && s < 6) {
          if (s == 0) wait_vm8<4>(R[1]);
          if (s == 1) wait_vm8<10>(R[2]);
          if (s == 2) wait_vm8<10>(R[0]);
          if (s == 3) wait_vm8<10>(R[1]);
          if (s == 4) wait_vm8<13>(R[2]);
          if (s == 5) wait_vm8<0>(R[0]);
        }
        if (qt == 0 && s == 4) { kdma(9); kdma(10); kdma(11); }
        if (qt < 4 && s < 5) { wld(s + 2, 2 * qt); wld(s + 2, 2 * qt + 1); }
        if (qt >= 4 && s == 0) { for (int n = 0; n < 3; ++n) xdma(line_of(1), 6 * half + 3 * (qt - 4) + n); }
        if (qt >= 4 && s == 1) { for (int n = 0; n < 3; ++n) xdma(line_of(2), 6 * half + 3 * (qt - 4) + n); }
        if (qt == 4 && s == 2) { for (int n = 0; n < 3; ++n) xdma(6, 3 * wave + n); }
        if (qt == 5 && s == 2) { kdma(0); kdma(1); kdma(2); }
        if (qt >= 4 && s == 3) { kdma(3 + 3 * (qt - 4)); kdma(4 + 3 * (qt - 4)); kdma(5 + 3 * (qt - 4)); }
        if (qt >= 2 && s < 6) transpose_q(R[(s + 1) % 3], F[(s + 1) & 1], qt - 2);
#pragma unroll
        for (int c = 0; c < 8; ++c) acc[c][qt] = MFMA(F[s & 1][c], xb[qt & 1], acc[c][qt]);
        if (!(s == 6)) SB();
        if (bar_after) {
          if (s == 1) wait_vm<14>();
          __syncthreads();
          xb[0] = *(const u32x4*)xaddr(s + 1, 0);
        }
      }
    }
#pragma unroll
    for (int bb = 0; bb < 2; ++bb) {
      u32x4 kf[3][4];
#pragma unroll
      for (int kt = 0; kt < 3; ++kt)
#pragma unroll
        for (int s = 0; s < 4; ++s) {
          const int row = 48 * bb + 16 * kt + i;
          kf[kt][s] = *(const u32x4*)(lds + LDS_K + row * 512 + (((16 * half + 4 * gq + s) ^ swap02(i)) * 16));
        }
#pragma unroll
      for (int qt = 0; qt < 3; ++qt) {
        const int q6 = 3 * bb + qt;
        f32x4 sacc[3];
#pragma unroll
        for (int kt = 0; kt < 3; ++kt) sacc[kt] = f32x4{0.f, 0.f, 0.f, 0.f};
#pragma unroll
        for (int s = 0; s < 4; ++s) {
          u32x4 qf = {pk2(acc[0][q6][s], acc[1][q6][s]), pk2(acc[2][q6][s], acc[3][q6][s]),
                      pk2(acc[4][q6][s], acc[5][q6][s]), pk2(acc[6][q6][s], acc[7][q6][s])};
#pragma unroll
          for (int kt = 0; kt < 3; ++kt) sacc[kt] = MFMA(kf[kt][s], qf, sacc[kt]);
        }
#pragma unroll
        for (int kt = 0; kt < 3; ++kt)
          *(f32x4*)(lds + LDS_X + (wave * 18 + (bb * 3 + qt) * 3 + kt) * 1024 + lane * 16) = sacc[kt];
      }
    }
    __syncthreads();
    {
      f32x4 s[2][3];
      const int u1 = wave < 2 ? wave + 4 : wave;
#pragma unroll
      for (int rep = 0; rep < 2; ++rep) {
        const int u = rep ? u1 : wave;
#pragma unroll
        for (int kt = 0; kt < 3; ++kt) {
          s[rep][kt] = *(const f32x4*)(lds + LDS_X + (0 * 18 + u * 3 + kt) * 1024 + lane * 16);
#pragma unroll
          for (int ww = 1; ww < 4; ++ww) s[rep][kt] += *(const f32x4*)(lds + LDS_X + (ww * 18 + u * 3 + kt) * 1024 + lane * 16);
        }
      }
      const float sc = 0.0625f * 1.44269504088896340736f;
#pragma unroll
      for (int rep = 0; rep < 2; ++rep) {
        float m = -INFINITY;
#pragma unroll
        for (int kt = 0; kt < 3; ++kt)
#pragma unroll
          for (int r = 0; r < 4; ++r) m = fmaxf(m, s[rep][kt][r]);
        m = red_max(m) * sc;
        float sum = 0.f;
#pragma unroll
        for (int kt = 0; kt < 3; ++kt)
#pragma unroll
          for (int r = 0; r < 4; ++r) {
            s[rep][kt][r] = __builtin_amdgcn_exp2f(__builtin_fmaf(s[rep][kt][r], sc, -m));
            sum += s[rep][kt][r];
          }
        sum = red_sum(sum);
        const float inv = __builtin_amdgcn_rcpf(sum);
#pragma unroll
        for (int kt = 0; kt < 3; ++kt) s[rep][kt] *= inv;
      }
#pragma unroll
      for (int rep = 0; rep < 2; ++rep) {
        if (rep == 0 || wave < 2) {
          const int u = rep ? u1 : wave;
          const int bb = u >= 3 ? 1 : 0, qt = u - 3 * bb;
          const int b = 2 * p + bb;
          st16_wt(ws + WS_PA + (size_t)((b * HQ + h) * 3 + qt) * 1024 + lane * 16, cat(pk4(s[rep][0]), pk4(s[rep][1])));
          st8_wt(ws + WS_PB + (size_t)((b * 8 + g) * 3 + qt) * 1024 + (((2 * (h & 1) + (gq >> 1)) << 4) | i) * 16 + (gq & 1) * 8, pk4(s[rep][2]));
        }
      }
    }
  } else {
    const int j = 4 * (idx & 1) + wave;
    const bool active = j < 7;
    auto line_of = [&](int t) { return ROT ? (t + p) & 3 : t; };
    const unsigned voff0 = (unsigned)(l8 * 512 + ((e8 ^ (l8 >> 1)) * 16));
    const unsigned voff1 = (unsigned)(l8 * 512 + ((e8 ^ ((l8 >> 1) | 4)) * 16));
    auto vdma = [&](int d, int n) {
      const int q = 3 * wave + n;
      const int t = 2 * (d & 1) + (q >= 6 ? 1 : 0);
      const int rgl = q >= 6 ? q - 6 : q;
      const int bb = d >> 1;
      const int L = line_of(t);
      const char* vs_ = (const char*)(vc + (size_t)((2 * p + bb) * HKV + g) * SK * DH);
      dma16nt(lds0 + LDS_V + L * 12288 + (6 * bb + rgl) * 1024, vs_ + (size_t)rgl * 8 * 512 + L * 128, (rgl & 1) ? voff1 : voff0);
    };
    if (!active) {
      unsigned sink = 0;
#pragma unroll
      for (int n = 0; n < 4; ++n) {
        const int ln = n * 64 + lane;
        touch(sink, (const char*)pc0 + (size_t)(ln < CODE_LINES ? ln : 0) * 128);
      }
#pragma unroll
      for (int d = 0; d < 4; ++d)
#pragma unroll
        for (int n = 0; n < 3; ++n) vdma(d, n);
      wait_vm<9>();
      asm volatile("" : "+v"(sink));
      __syncthreads();
      wait_vm<6>();
      __syncthreads();
      wait_vm<0>();
      __syncthreads();
    } else {
      const int np = 128 * j + 8 * i;
      const int hl = np >= DM ? 1 : 0;
      const int n0 = np - DM * hl;
      const unsigned woffa = (unsigned)((hl * DH + 8 * gq) * DM + n0) * 2u;
      const unsigned woffb = woffa + 4 * DM * 2;
      const char* wbase = (const char*)(wo + (size_t)(2 * g) * DH * DM);
      u32x4 R[3][8];
      auto wld = [&](int s, int jr) {
        const char* sb = wbase + (size_t)(32 * (2 * line_of(s >> 1) + (s & 1))) * (DM * 2);
        if (jr == 0) gld16o<0>(R[s % 3][0], sb, woffa);
        if (jr == 1) gld16o<896>(R[s % 3][1], sb, woffa);
        if (jr == 2) gld16o<1792>(R[s % 3][2], sb, woffa);
        if (jr == 3) gld16o<2688>(R[s % 3][3], sb, woffa);
        if (jr == 4) gld16o<0>(R[s % 3][4], sb, woffb);
        if (jr == 5) gld16o<896>(R[s % 3][5], sb, woffb);
        if (jr == 6) gld16o<1792>(R[s % 3][6], sb, woffb);
        if (jr == 7) gld16o<2688>(R[s % 3][7], sb, woffb);
      };
      auto vaddr = [&](int s, int mt) {
        const int row = 16 * mt + i;
        return lds + LDS_V + line_of(s >> 1) * 12288 + row * 128 + (((4 * (s & 1) + gq) ^ ((row >> 1) & 7)) * 16);
      };
      const int m = n0 >> 6, ip = (n0 >> 3) & 7;
      const int h = 2 * g + hl;
      char* va_base = ws + WS_VA + (size_t)(((2 * p * HQ + h) * 7 + m) * 4) * 1024 + ((gq << 4) | ip) * 16;
      char* vb_base = ws + WS_VB + (size_t)(((2 * p * 8 + g) * 7 + m) * 4) * 1024 + (((2 * hl + (gq >> 1)) << 4) | ((gq & 1) << 3) | ip) * 16;
      auto store_c2 = [&](f32x4 (&acc)[3][8], int bb, int c2) {
#pragma unroll
        for (int e = 0; e < 2; ++e)
          st16_wt(va_base + (size_t)bb * HQ * 28 * 1024 + c2 * 1024 + e * 128, cat(pk4(acc[0][2 * c2 + e]), pk4(acc[1][2 * c2 + e])));
        u32x2 a = pk4(acc[2][2 * c2]), bq = pk4(acc[2][2 * c2 + 1]);
        auto r0 = __builtin_amdgcn_permlane16_swap(a[0], bq[0], false, false);
        auto r1 = __builtin_amdgcn_permlane16_swap(a[1], bq[1], false, false);
        u32x4 t = {(unsigned)r0[0], (unsigned)r1[0], (unsigned)r0[1], (unsigned)r1[1]};
        st16_wt(vb_base + (size_t)bb * 8 * 28 * 1024 + c2 * 1024, t);
      };
#pragma unroll
      for (int n = 0; n < 3; ++n) vdma(0, n);
#pragma unroll
      for (int jr = 0; jr < 8; ++jr) wld(0, jr);
      wait_vm<8>();
      __syncthreads();
#pragma unroll
      for (int jr = 0; jr < 8; ++jr) wld(1, jr);
      f32x4 acc0[3][8], acc1[3][8];
#pragma unroll
      for (int mt = 0; mt < 3; ++mt)
#pragma unroll
        for (int c = 0; c < 8; ++c) { acc0[mt][c] = f32x4{0.f, 0.f, 0.f, 0.f}; acc1[mt][c] = f32x4{0.f, 0.f, 0.f, 0.f}; }
      u32x4 F[8][8];
      u32x4 va[2];
      va[0] = *(const u32x4*)vaddr(0, 0);
      wait_vm8<8>(R[0]);
      transpose8(R[0], F[0]);
      SB();
#pragma unroll
      for (int s = 0; s < 8; ++s) {
#pragma unroll
        for (int mt = 0; mt < 3; ++mt) {
          const int gidx = 3 * s + mt;
          const bool bar_after = (mt == 2) && (s == 3 || s == 7);
          if (mt < 2) va[(gidx + 1) & 1] = *(const u32x4*)vaddr(s, mt + 1);
          else if (!bar_after) va[(gidx + 1) & 1] = *(const u32x4*)vaddr(s + 1, 0);
          if (mt == 1 && s < 7) {
            if (s == 0) wait_vm8<3>(R[1]);
            if (s == 1) wait_vm8<6>(R[2]);
            if (s == 2) wait_vm8<3>(R[0]);
            if (s == 3) wait_vm8<3>(R[1]);
            if (s == 4) wait_vm8<3>(R[2]);
            if (s == 5) wait_vm8<3>(R[0]);
            if (s == 6) wait_vm8<3>(R[1]);
          }
          if (s < 6) {
            if (mt == 0) { wld(s + 2, 0); wld(s + 2, 1); wld(s + 2, 2); }
            if (mt == 1) { wld(s + 2, 3); wld(s + 2, 4); wld(s + 2, 5); }
            if (mt == 2) { wld(s + 2, 6); wld(s + 2, 7); }
          }
          if (mt == 2 && s == 0) { for (int n = 0; n < 3; ++n) vdma(1, n); }
          if (mt == 2 && (s == 5 || s == 6)) { for (int n = 0; n < 3; ++n) vdma(s - 3, n); }
          if (mt >= 1 && s < 7) { transpose_q(R[(s + 1) % 3], F[s + 1], 2 * (mt - 1)); transpose_q(R[(s + 1) % 3], F[s + 1], 2 * (mt - 1) + 1); }
#pragma unroll
          for (int c = 0; c < 8; ++c) acc0[mt][c] = MFMA(va[gidx & 1], F[s][c], acc0[mt][c]);
          SB();
          if (bar_after) {
            if (s == 7) wait_vm<0>();
            __syncthreads();
            if (s != 7) va[(gidx + 1) & 1] = *(const u32x4*)vaddr(s + 1, 0);
          }
        }
        }
      {
        u32x4 vb[3][3];
#pragma unroll
        for (int mt = 0; mt < 3; ++mt) { vb[0][mt] = *(const u32x4*)vaddr(0, 3 + mt); vb[1][mt] = *(const u32x4*)vaddr(1, 3 + mt); }
#pragma unroll
        for (int c2 = 0; c2 < 4; ++c2) {
#pragma unroll
          for (int s = 0; s < 8; ++s) {
            const int n = c2 * 8 + s;
            if (n + 2 < 32) {
#pragma unroll
              for (int mt = 0; mt < 3; ++mt) vb[(n + 2) % 3][mt] = *(const u32x4*)vaddr((s + 2) & 7, 3 + mt);
            }
            if (s == 0) store_c2(acc0, 0, c2);
            if (s == 4 && c2 > 0) store_c2(acc1, 1, c2 - 1);
#pragma unroll
            for (int mt = 0; mt < 3; ++mt)
#pragma unroll
              for (int e = 0; e < 2; ++e) acc1[mt][2 * c2 + e] = MFMA(vb[n % 3][mt], F[s][2 * c2 + e], acc1[mt][2 * c2 + e]);
            SB();
          }
        }
      }
      store_c2(acc1, 1, 3);
    }
  }

  asm volatile("s_waitcnt vmcnt(0)" ::: "memory");
  __syncthreads();
  {
    const int prod = idx < 16 ? 2 * g + (idx & 1) : 16 + 2 * g + (idx & 1);
    if (wave == 0 && lane < 14) {
      unsigned long long* f = (unsigned long long*)(ws + WS_FL) + ((size_t)(p * 14 + lane) * 32 + prod);
      __hip_atomic_store(f, MAGIC, __ATOMIC_RELAXED, __HIP_MEMORY_SCOPE_AGENT);
    }
  }
  if (idx >= 16 && idx < 30) {
    const int ib = idx - 16;
    const int tb = g + 8 * (ib / 7), tm = ib % 7;
    const int tp = tb >> 1;
    unsigned long long* fl = (unsigned long long*)(ws + WS_FL) + (size_t)(tp * 14 + (tb & 1) * 7 + tm) * 32;
    unsigned long long* myf = fl + (lane < 4 ? 16 + 4 * wave + lane : 4 * wave + (lane & 3));
    auto poll = [&](unsigned long long need) {
      unsigned long long got = 0;
      for (unsigned spin = 0; spin < (1u << 18); ++spin) {
        unsigned long long v = 0;
        if (lane < 8) v = __hip_atomic_load(myf, __ATOMIC_RELAXED, __HIP_MEMORY_SCOPE_AGENT);
        got = __ballot(v == MAGIC);
        if ((got & need) == need) break;
      }
      return got;
    };
    u32x4 pf[2][3][3], wf[2][3][4];
    const unsigned lo = lane * 16;
    const bool a_ready = (poll(0x0full) & 0xf0ull) == 0xf0ull;
#pragma unroll
    for (int hp2 = 0; hp2 < 2; ++hp2) {
      const int hp = 2 * wave + hp2;
#pragma unroll
      for (int c2 = 0; c2 < 4; ++c2) {
        gld16c(wf[hp2][0][c2], ws + WS_VA + (size_t)(((tb * HQ + 2 * hp) * 7 + tm) * 4 + c2) * 1024, lo);
        gld16c(wf[hp2][1][c2], ws + WS_VA + (size_t)(((tb * HQ + 2 * hp + 1) * 7 + tm) * 4 + c2) * 1024, lo);
        gld16c(wf[hp2][2][c2], ws + WS_VB + (size_t)(((tb * 8 + hp) * 7 + tm) * 4 + c2) * 1024, lo);
      }
    }
    if (!a_ready) poll(0xf0ull);
#pragma unroll
    for (int qt = 0; qt < 3; ++qt)
#pragma unroll
      for (int hp2 = 0; hp2 < 2; ++hp2) {
        const int hp = 2 * wave + hp2;
        gld16c(pf[hp2][0][qt], ws + WS_PA + (size_t)((tb * HQ + 2 * hp) * 3 + qt) * 1024, lo);
        gld16c(pf[hp2][1][qt], ws + WS_PA + (size_t)((tb * HQ + 2 * hp + 1) * 3 + qt) * 1024, lo);
        gld16c(pf[hp2][2][qt], ws + WS_PB + (size_t)((tb * 8 + hp) * 3 + qt) * 1024, lo);
      }
    asm volatile("s_waitcnt vmcnt(18)"
                 : "+v"(wf[0][0][0]), "+v"(wf[0][0][1]), "+v"(wf[0][0][2]), "+v"(wf[0][0][3]), "+v"(wf[0][1][0]), "+v"(wf[0][1][1]),
                   "+v"(wf[0][1][2]), "+v"(wf[0][1][3]), "+v"(wf[0][2][0]), "+v"(wf[0][2][1]), "+v"(wf[0][2][2]), "+v"(wf[0][2][3]),
                   "+v"(wf[1][0][0]), "+v"(wf[1][0][1]), "+v"(wf[1][0][2]), "+v"(wf[1][0][3]), "+v"(wf[1][1][0]), "+v"(wf[1][1][1]),
                   "+v"(wf[1][1][2]), "+v"(wf[1][1][3]), "+v"(wf[1][2][0]), "+v"(wf[1][2][1]), "+v"(wf[1][2][2]), "+v"(wf[1][2][3])
                 :: "memory");
#pragma unroll
    for (int qt = 0; qt < 3; ++qt) {
      asm volatile("s_waitcnt vmcnt(%6)"
                   : "+v"(pf[0][0][qt]), "+v"(pf[0][1][qt]), "+v"(pf[0][2][qt]), "+v"(pf[1][0][qt]), "+v"(pf[1][1][qt]), "+v"(pf[1][2][qt])
                   : "i"(12 - 6 * qt));
#pragma unroll
      for (int c2 = 0; c2 < 4; ++c2) {
        f32x4 a = f32x4{0.f, 0.f, 0.f, 0.f};
#pragma unroll
        for (int hp2 = 0; hp2 < 2; ++hp2)
#pragma unroll
          for (int u = 0; u < 3; ++u) a = MFMA(pf[hp2][u][qt], wf[hp2][u][c2], a);
        *(f32x4*)(lds + ((wave * 3 + qt) * 4 + c2) * 1024 + lane * 16) = a;
      }
    }
    if (lane < 8) __hip_atomic_store(myf, 0ull, __ATOMIC_RELAXED, __HIP_MEMORY_SCOPE_AGENT);
    __syncthreads();
    const int tid = threadIdx.x;
#pragma unroll
    for (int rep = 0; rep < 2; ++rep) {
      const int t = tid + 256 * rep;
      if (t < 384) {
        const int q = t >> 3, ip = t & 7;
        const int qt = q >> 4, g4 = (q >> 2) & 3, r = q & 3;
        float v[8];
#pragma unroll
        for (int c = 0; c < 8; ++c) v[c] = 0.f;
#pragma unroll
        for (int ww = 0; ww < 4; ++ww)
#pragma unroll
          for (int c = 0; c < 8; ++c)
            v[c] += *(const float*)(lds + ((ww * 3 + qt) * 4 + (c >> 1)) * 1024 + ((g4 << 4) | ((c & 1) << 3) | ip) * 16 + r * 4);
        u32x4 o = {pk2(v[0], v[1]), pk2(v[2], v[3]), pk2(v[4], v[5]), pk2(v[6], v[7])};
        *(u32x4*)(out + (size_t)(tb * SQ + q) * DM + 64 * tm + 8 * ip) = o;
      }
    }
  }
}

extern "C" void kernel_launch(void* const* d_in, const int* in_sizes, int n_in, void* d_out, int out_size, void* d_ws,
                              size_t ws_size, hipStream_t stream) {
  const uint16_t* x = (const uint16_t*)d_in[0];
  const uint16_t* kc = (const uint16_t*)d_in[1];
  const uint16_t* vc = (const uint16_t*)d_in[2];
  const uint16_t* wq = (const uint16_t*)d_in[3];
  const uint16_t* wo = (const uint16_t*)d_in[4];
  k1_attn<<<256, 256, 0, stream>>>(x, kc, vc, wq, wo, (char*)d_ws, (uint16_t*)d_out);
}
```
